```python
import torch

def _softmax(x, axis=-1):
    return torch.softmax(x, dim=axis)

def _gelu(x):
    return torch.nn.functional.gelu(x, approximate='tanh')

def _silu(x):
    return torch.nn.functional.silu(x)

def _sigmoid(x):
    return torch.sigmoid(x)

def _layernorm(x, axis=-1, eps=1e-05):
    m = x.mean(dim=axis, keepdim=True)
    v = x.var(dim=axis, keepdim=True, unbiased=False)
    return (x - m) / torch.sqrt(v + eps)

def _scatter_add(tgt, idx, src, axis=0):
    return tgt.clone().index_add_(axis, idx, src)

def _segment_sum(src, seg, n):
    out = torch.zeros((n,) + src.shape[1:], dtype=src.dtype)
    return out.index_add_(0, seg, src)

def _pad(x, pw):
    flat = tuple((v for pair in reversed(pw) for v in pair))
    return torch.nn.functional.pad(x, flat)
kernel_name = 'gated_mix__ssm_conv_s5__mixer_gmlp__prompt'

def setup_inputs():

    _w = lambda a: (lambda t: t.to(torch.bfloat16) if t.is_floating_point() else t)(torch.as_tensor(a))

    def _A_setup():
        B, S, D = (1, 2048, 512)
        P = D
        Lambda_re = -0.5 * (lambda _x: torch.exp(torch.as_tensor(_x)))((lambda *s: torch.randn(*s, dtype=torch.bfloat16) * 0.8)(P))
        Lambda_im = (lambda *s: torch.randn(*s, dtype=torch.bfloat16) * 0.8)(P) * 1.0
        B_re = (lambda *s: torch.randn(*s, dtype=torch.bfloat16) * 0.8)(P, D) * D ** (-0.5)
        B_im = (lambda *s: torch.randn(*s, dtype=torch.bfloat16) * 0.8)(P, D) * D ** (-0.5)
        C_re = (lambda *s: torch.randn(*s, dtype=torch.bfloat16) * 0.8)(D, P) * P ** (-0.5)
        C_im = (lambda *s: torch.randn(*s, dtype=torch.bfloat16) * 0.8)(D, P) * P ** (-0.5)
        log_dt = (lambda *s: torch.randn(*s, dtype=torch.bfloat16) * 0.8)(P) * 0.3 - 2.0
        Dskip = (lambda *s: torch.randn(*s, dtype=torch.bfloat16) * 0.8)(D)
        return {'x': _w((lambda *s: torch.randn(*s, dtype=torch.bfloat16) * 0.05)(B, S, D)), 'Lambda_re': _w(Lambda_re), 'Lambda_im': _w(Lambda_im), 'B_re': _w(B_re), 'B_im': _w(B_im), 'C_re': _w(C_re), 'C_im': _w(C_im), 'log_dt': _w(log_dt), 'Dskip': _w(Dskip)}
    _A_in = _A_setup()

    def _B_setup():
        B, S, D, F = (1, 2048, 512, 1376)
        Fh = F // 2
        return {'x': _w((lambda *s: torch.randn(*s, dtype=torch.bfloat16) * 0.05)(B, S, D)), 'w_in': _w((lambda *s: torch.randn(*s, dtype=torch.bfloat16) * 0.2)(D, 2 * Fh)), 'w_sp': _w(0.01 * (lambda *s: torch.randn(*s, dtype=torch.bfloat16) * 0.2)(S, S)), 'b_sp': _w(1.0 + 0.01 * (lambda *s: torch.randn(*s, dtype=torch.bfloat16) * 0.05)(S)), 'w_out': _w((lambda *s: torch.randn(*s, dtype=torch.bfloat16) * 0.05)(Fh, D))}
    _B_in = _B_setup()
    return {'A_x': _A_in['x'], 'A_Lambda_re': _A_in['Lambda_re'], 'A_Lambda_im': _A_in['Lambda_im'], 'A_B_re': _A_in['B_re'], 'A_B_im': _A_in['B_im'], 'A_C_re': _A_in['C_re'], 'A_C_im': _A_in['C_im'], 'A_log_dt': _A_in['log_dt'], 'A_Dskip': _A_in['Dskip'], 'B_x': _B_in['x'], 'B_w_in': _B_in['w_in'], 'B_w_sp': _B_in['w_sp'], 'B_b_sp': _B_in['b_sp'], 'B_w_out': _B_in['w_out'], 'gate_w': _w((lambda *s: torch.randn(*s, dtype=torch.bfloat16) * 0.05)(512) * 16.0)}

def reference(A_x, A_Lambda_re, A_Lambda_im, A_B_re, A_B_im, A_C_re, A_C_im, A_log_dt, A_Dskip, B_x, B_w_in, B_w_sp, B_b_sp, B_w_out, gate_w):

    def _A_ref(x, Lambda_re, Lambda_im, B_re, B_im, C_re, C_im, log_dt, Dskip):
        Bsz, L, H = x.shape
        Lam = Lambda_re + 1j * Lambda_im
        Bmat = B_re + 1j * B_im
        Cmat = C_re + 1j * C_im
        dt = torch.exp(log_dt)
        Lam_bar = torch.exp(dt * Lam)
        B_bar = ((Lam_bar - 1.0) / Lam)[:, None] * Bmat
        Bu = torch.einsum('bld,pd->blp', x + 0j, B_bar)
        h = torch.zeros_like(Bu[:, 0, :])
        hs = []
        for t in range(L):
            h = Lam_bar[None, :] * h + Bu[:, t, :]
            hs.append(h)
        hseq = torch.stack(hs, 1)
        y = 2.0 * torch.einsum('blp,dp->bld', hseq, Cmat).real + x * Dskip[None, None, :]
        return y

    def _B_ref(x, w_in, w_sp, b_sp, w_out):
        B, S, D = x.shape
        F = w_in.shape[-1]
        Fh = F // 2
        z = _gelu(torch.einsum('bsd,df->bsf', x, w_in))
        z1 = z[..., :Fh].clone()
        z2 = _layernorm(z[..., Fh:], axis=-1)
        g = torch.einsum('ts,bsf->btf', w_sp, z2) + b_sp[None, :, None]
        return torch.einsum('bsf,fd->bsd', z1 * g, w_out)
    x = A_x
    g = _sigmoid((x * gate_w).sum(-1, keepdims=True))
    a = _A_ref(x, A_Lambda_re, A_Lambda_im, A_B_re, A_B_im, A_C_re, A_C_im, A_log_dt, A_Dskip)
    b = _B_ref(x, B_w_in, B_w_sp, B_b_sp, B_w_out)
    return g * a + (1 - g) * b
```

```cpp
#include <hip/hip_runtime.h>
#include <hip/hip_bf16.h>
#include <stdint.h>
typedef unsigned short u16;
static __device__ inline float b2f(u16 v){ return __uint_as_float(((unsigned)v)<<16); }
static __device__ inline u16 f2b(float f){ __hip_bfloat16 h = __float2bfloat16(f); return *(u16*)&h; }
constexpr int S=2048,D=512,P=512,F=1376,FH=688;

__global__ void k_u(const u16* x,const u16* Bre,const u16* Bim,const u16* lre,const u16* lim,const u16* ldt,float* bu){
  int p=blockIdx.x*64+threadIdx.x, t=blockIdx.y;
  float ar=0,ai=0;
  for(int d=0;d<D;d++){ float xv=b2f(x[t*D+d]); ar+=xv*b2f(Bre[p*D+d]); ai+=xv*b2f(Bim[p*D+d]); }
  float dt=b2f(f2b(expf(b2f(ldt[p])))); float lr=b2f(lre[p]), li=b2f(lim[p]);
  float e=expf(dt*lr), c=cosf(dt*li), s=sinf(dt*li);
  float nr=e*c-1.f, ni=e*s; float den=lr*lr+li*li;
  float cr=(nr*lr+ni*li)/den, ci=(ni*lr-nr*li)/den;
  bu[(t*P+p)*2]=cr*ar-ci*ai; bu[(t*P+p)*2+1]=cr*ai+ci*ar;
}
__global__ void k_scan(const u16* lre,const u16* lim,const u16* ldt,float* bu){
  int p=blockIdx.x*64+threadIdx.x;
  float dt=b2f(f2b(expf(b2f(ldt[p])))); float lr=b2f(lre[p]), li=b2f(lim[p]);
  float e=expf(dt*lr), a=e*cosf(dt*li), b=e*sinf(dt*li);
  float hr=0,hi=0;
  for(int t=0;t<S;t++){ float ur=bu[(t*P+p)*2], ui=bu[(t*P+p)*2+1]; float nr=a*hr-b*hi+ur, ni=a*hi+b*hr+ui; hr=nr;hi=ni; bu[(t*P+p)*2]=hr; bu[(t*P+p)*2+1]=hi; }
}
__global__ void k_z(const u16* x,const u16* win,float* z){
  int f=blockIdx.x*32+threadIdx.x, t=blockIdx.y;
  float a=0; for(int d=0;d<D;d++) a+=b2f(x[t*D+d])*b2f(win[d*F+f]);
  float u=0.7978845608f*(a+0.044715f*a*a*a); z[t*F+f]=0.5f*a*(1.f+tanhf(u));
}
__global__ void k_ln(float* z){
  int t=blockIdx.x*64+threadIdx.x; float m=0,v=0;
  for(int f=0;f<FH;f++) m+=z[t*F+FH+f]; m/=FH;
  for(int f=0;f<FH;f++){ float d=z[t*F+FH+f]-m; v+=d*d;} v/=FH; float r=1.f/sqrtf(v+1e-5f);
  for(int f=0;f<FH;f++) z[t*F+FH+f]=(z[t*F+FH+f]-m)*r;
}
__global__ void k_g(const u16* wsp,const u16* bsp,const float* z,float* v){
  int f=blockIdx.x*16+threadIdx.x, t=blockIdx.y;
  float a=0; for(int s=0;s<S;s++) a+=b2f(wsp[t*S+s])*z[s*F+FH+f];
  v[t*FH+f]=z[t*F+f]*(a+b2f(bsp[t]));
}
__global__ void k_o(const u16* x,const u16* gw,const u16* Cre,const u16* Cim,const u16* dsk,const u16* wout,const float* h,const float* v,float* out){
  int d=blockIdx.x*64+threadIdx.x, t=blockIdx.y;
  float gs=0; for(int k=0;k<D;k++) gs+=b2f(x[t*D+k])*b2f(gw[k]);
  float g=1.f/(1.f+expf(-gs));
  float a=0; for(int p=0;p<P;p++) a+=h[(t*P+p)*2]*b2f(Cre[d*P+p])-h[(t*P+p)*2+1]*b2f(Cim[d*P+p]);
  a=2.f*a+b2f(x[t*D+d])*b2f(dsk[d]);
  float b=0; for(int f=0;f<FH;f++) b+=v[t*FH+f]*b2f(wout[f*D+d]);
  out[t*D+d]=g*a+(1.f-g)*b;
}
extern "C" void kernel_launch(void* const* d_in, const int* in_sizes, int n_in, void* d_out, int out_size, void* d_ws, size_t ws_size, hipStream_t stream) {
  const u16* x=(const u16*)d_in[0]; const u16* lre=(const u16*)d_in[1]; const u16* lim=(const u16*)d_in[2];
  const u16* Bre=(const u16*)d_in[3]; const u16* Bim=(const u16*)d_in[4]; const u16* Cre=(const u16*)d_in[5]; const u16* Cim=(const u16*)d_in[6];
  const u16* ldt=(const u16*)d_in[7]; const u16* dsk=(const u16*)d_in[8]; const u16* win=(const u16*)d_in[10];
  const u16* wsp=(const u16*)d_in[11]; const u16* bsp=(const u16*)d_in[12]; const u16* wout=(const u16*)d_in[13]; const u16* gw=(const u16*)d_in[14];
  float* bu=(float*)d_ws; float* z=bu+S*P*2; float* v=z+S*F;
  k_u<<<dim3(P/64,S),64,0,stream>>>(x,Bre,Bim,lre,lim,ldt,bu);
  k_scan<<<P/64,64,0,stream>>>(lre,lim,ldt,bu);
  k_z<<<dim3(F/32,S),32,0,stream>>>(x,win,z);
  k_ln<<<S/64,64,0,stream>>>(z);
  k_g<<<dim3(FH/16,S),16,0,stream>>>(wsp,bsp,z,v);
  k_o<<<dim3(D/64,S),64,0,stream>>>(x,gw,Cre,Cim,dsk,wout,bu,v,(float*)d_out);
}
```

```cpp
#include <hip/hip_runtime.h>
#include <stdint.h>

namespace {
typedef unsigned long long u64;
typedef unsigned short u16;
typedef unsigned u32;
typedef short bf16x8 __attribute__((ext_vector_type(8)));
typedef short s16x4 __attribute__((ext_vector_type(4)));
typedef float f32x4 __attribute__((ext_vector_type(4)));
typedef float f32x2 __attribute__((ext_vector_type(2)));
typedef __bf16 bf2 __attribute__((ext_vector_type(2)));
typedef int i32x4 __attribute__((ext_vector_type(4)));
typedef u32 u32x4 __attribute__((ext_vector_type(4)));
typedef u32 u32x2 __attribute__((ext_vector_type(2)));
#define LAS __attribute__((address_space(3)))
#define GAS __attribute__((address_space(1)))
#define DI __device__ __forceinline__

constexpr int FH = 688;
constexpr u32 SLOT = 40960;
constexpr u32 L_GW = 3 * SLOT, L_STAT = L_GW + 1024, L_GATE = L_STAT + 512, L_CARRY = L_GATE + 256;
constexpr u32 L_POW = L_CARRY + 1024;
constexpr u32 LDS_BYTES = L_POW + 3072;

constexpr u32 WS_EPOCH = 0;
constexpr u32 WS_F1 = 4096, WS_FW = WS_F1 + 8192, WS_F2 = WS_FW + 8192, WS_F3 = WS_F2 + 8192;
constexpr u32 WS_STAT = 65536;
constexpr u32 WS_SEG = WS_STAT + 32 * 16 * 64 * 16;
constexpr u32 WS_Z = 1u << 20;
constexpr u32 WS_H = WS_Z + 64 * 48 * 1024;
constexpr u32 WS_V = WS_H + 2 * 64 * 32 * 1024;
constexpr u32 WS_END = WS_V + 64 * 48 * 1024;
constexpr u32 WS_STAMP = 16u << 20;
constexpr u64 SPIN_TICKS = 2000000;

DI i32x4 mkrsrc(const void* p, u32 bytes) {
  u64 a = (u64)p;
  i32x4 r;
  r[0] = __builtin_amdgcn_readfirstlane((u32)a);
  r[1] = __builtin_amdgcn_readfirstlane((u32)(a >> 32) & 0xffff);
  r[2] = bytes;
  r[3] = 0x00020000;
  return r;
}
template <int SC1>
DI void dma(u32 lds, u32 voff, i32x4 rs, u32 soff) {
  if constexpr (SC1)
    asm volatile("s_mov_b32 m0, %0\n\ts_nop 0\n\tbuffer_load_dwordx4 %1, %2, %3 offen sc1 lds" ::"s"(lds), "v"(voff), "s"(rs), "s"(soff) : "memory");
  else
    asm volatile("s_mov_b32 m0, %0\n\ts_nop 0\n\tbuffer_load_dwordx4 %1, %2, %3 offen lds" ::"s"(lds), "v"(voff), "s"(rs), "s"(soff) : "memory");
}
DI u32x4 ld16_issue(u32 voff, i32x4 rs) {
  u32x4 r;
  asm volatile("buffer_load_dwordx4 %0, %1, %2, 0 offen sc1" : "=v"(r) : "v"(voff), "s"(rs) : "memory");
  return r;
}
DI u32x2 ld8_issue(u32 voff, i32x4 rs) {
  u32x2 r;
  asm volatile("buffer_load_dwordx2 %0, %1, %2, 0 offen sc1" : "=v"(r) : "v"(voff), "s"(rs) : "memory");
  return r;
}
#define WAITV(n) asm volatile("s_waitcnt vmcnt(%0)" ::"n"(n) : "memory")
#define WAITV1(n, a) asm volatile("s_waitcnt vmcnt(%1)" : "+v"(a) : "n"(n) : "memory")
#define WAITV4(n, a, b, c, d) asm volatile("s_waitcnt vmcnt(%4)" : "+v"(a), "+v"(b), "+v"(c), "+v"(d) : "n"(n) : "memory")
#define LBAR() asm volatile("s_waitcnt lgkmcnt(0)\n\ts_barrier" ::: "memory")
#define SB() __builtin_amdgcn_sched_barrier(0)
#define MFMA(a, b, c) __builtin_amdgcn_mfma_f32_16x16x32_bf16((a), (b), (c), 0, 0, 0)

DI bf16x8 lds16(u32 a) { return *(const LAS bf16x8*)(size_t)a; }
DI s16x4 lds8(u32 a) { return *(const LAS s16x4*)(size_t)a; }
DI s16x4 ldstr(u32 a) { return __builtin_amdgcn_ds_read_tr16_b64_v4i16((LAS s16x4*)(size_t)a); }
DI bf16x8 cat(s16x4 lo, s16x4 hi) { return __builtin_shufflevector(lo, hi, 0, 1, 2, 3, 4, 5, 6, 7); }
DI float bf2f(u16 v) { return __uint_as_float((u32)v << 16); }
DI u32 pk2(float a, float b) {
  f32x2 v = {a, b};
  return __builtin_bit_cast(u32, __builtin_convertvector(v, bf2));
}
DI float lo16(u32 v) { return __uint_as_float(v << 16); }
DI float hi16(u32 v) { return __uint_as_float(v & 0xffff0000u); }
DI float gelu(float x) {
  const float k0 = -2.302208198f, k1 = -0.1029432397f;
  float e = __builtin_amdgcn_exp2f(x * __builtin_fmaf(k1, x * x, k0));
  return x * __builtin_amdgcn_rcpf(1.0f + e);
}
struct cf { float re, im; };
DI cf cmul(cf a, cf b) { return {a.re * b.re - a.im * b.im, a.re * b.im + a.im * b.re}; }
DI cf cfma(cf a, cf b, cf c) {
  return {__builtin_fmaf(a.re, b.re, __builtin_fmaf(-a.im, b.im, c.re)), __builtin_fmaf(a.re, b.im, __builtin_fmaf(a.im, b.re, c.im))};
}
DI void sincos_cw(float x, float& s, float& c) {
  float k = __builtin_rintf(x * 0.6366197724f);
  float r = __builtin_fmaf(k, -1.57079637050628662109375f, x);
  r = __builtin_fmaf(k, 4.37113900018624283e-8f, r);
  float r2 = r * r;
  float ps = __builtin_fmaf(r2, __builtin_fmaf(r2, __builtin_fmaf(r2, 2.7557314297e-06f, -1.9841270114e-04f), 8.3333337680e-03f), -1.6666667163e-01f);
  float sr = __builtin_fmaf(r * r2, ps, r);
  float pc = __builtin_fmaf(r2, __builtin_fmaf(r2, __builtin_fmaf(r2, -2.7557314297e-07f, 2.4801587642e-05f), -1.3888889225e-03f), 4.1666667908e-02f);
  float cr = __builtin_fmaf(r2 * r2, pc, __builtin_fmaf(r2, -0.5f, 1.0f));
  int q = (int)k;
  float ss = (q & 1) ? cr : sr, cc = (q & 1) ? sr : cr;
  s = (q & 2) ? -ss : ss;
  c = ((q + 1) & 2) ? -cc : cc;
}
DI void pole(const u16* lre, const u16* lim, const u16* ldt, int p, cf& lam, cf& coef) {
  float lr = bf2f(lre[p]), li = bf2f(lim[p]);
  float dt = lo16(pk2(expf(bf2f(ldt[p])), 0.f));
  float e = expf(dt * lr), s, c;
  sincos_cw(dt * li, s, c);
  lam = {e * c, e * s};
  float inv = 1.0f / (lr * lr + li * li), nr = lam.re - 1.0f;
  coef = {(nr * lr + lam.im * li) * inv, (lam.im * lr - nr * li) * inv};
}
template <int CTRL>
DI float dpp(float v) {
  return __int_as_float(__builtin_amdgcn_update_dpp(0, __float_as_int(v), CTRL, 0xf, 0xf, true));
}
DI float reduce8(const float* v, int m) {
  float a[4], b[2];
  const bool h3 = m & 8, h2 = m & 4, h1 = m & 2;
#pragma unroll
  for (int k = 0; k < 4; k++) a[k] = (h3 ? v[k + 4] : v[k]) + dpp<0x140>(h3 ? v[k] : v[k + 4]);
#pragma unroll
  for (int k = 0; k < 2; k++) b[k] = (h2 ? a[k + 2] : a[k]) + dpp<0x141>(h2 ? a[k] : a[k + 2]);
  float c = (h1 ? b[1] : b[0]) + dpp<0x4E>(h1 ? b[0] : b[1]);
  return c + dpp<0xB1>(c);
}
DI bool flags_ok(u32x4 v, u32 lo, u32 hi, bool need) { return __all(!need || (v[0] == lo && v[1] == hi && v[2] == lo && v[3] == hi)); }
DI void spin_flags(u32 voff, i32x4 rs, u32 lo, u32 hi, bool need) {
  const u64 t0 = __builtin_amdgcn_s_memrealtime();
  for (;;) {
    u32x4 v = ld16_issue(voff, rs);
    WAITV1(0, v);
    if (flags_ok(v, lo, hi, need) || __builtin_amdgcn_s_memrealtime() - t0 > SPIN_TICKS) break;
    __builtin_amdgcn_s_sleep(2);
  }
}
DI void put_flag(char* ws, u32 off, u64 tag, int lane) {
  WAITV(0);
  if (lane == 0) __hip_atomic_store((GAS u64*)(ws + off), tag, __ATOMIC_RELAXED, __HIP_MEMORY_SCOPE_AGENT);
}
#define STAMP(k)

__global__ __launch_bounds__(512, 2) void fused_kernel(const u16* x, const u16* lre, const u16* lim, const u16* bre, const u16* bim,
                                                       const u16* cre, const u16* cim, const u16* ldt, const u16* dsk, const u16* win,
                                                       const u16* wsp, const u16* bsp, const u16* wout, const u16* gw, float* out, char* ws) {
  extern __shared__ __attribute__((aligned(16))) char smem[];
  const int tid = threadIdx.x, wave = __builtin_amdgcn_readfirstlane(tid >> 6), lane = tid & 63;
  const int xcd = blockIdx.x & 7, idx = blockIdx.x >> 3;
  const int i2 = (xcd & 3) * 8 + (idx & 7), j2 = (xcd >> 2) * 4 + (idx >> 3);
  const u32 sb = (u32)(size_t)(LAS char*)smem;
  const int m = lane & 15, g = lane >> 4;

  if (wave >= 4) {
    const int lw = wave - 4;
    const i32x4 rX = mkrsrc(x, 2048 * 512 * 2), rW = mkrsrc(win, 512 * 1376 * 2), rWS = mkrsrc(ws, WS_END);
    u32 vX[4], vW[6];
#pragma unroll
    for (int j = 0; j < 4; j++) vX[j] = (64 * i2 + 32 * (lw >> 1) + 8 * j + 4 * (lw & 1) + g) * 1024 + ((m ^ (4 * j + g)) << 4);
#pragma unroll
    for (int n = 0; n < 6; n++) {
      int c = n * 64 + lane, k = c / 12, cc = c - 12 * k;
      vW[n] = (32 * lw + k) * 2752 + 192 * j2 + ((cc ^ (((k >> 3) & 1) << 1)) << 4);
    }
    auto issueX = [&](u32 base, int s) {
#pragma unroll
      for (int j = 0; j < 4; j++) dma<0>(base + lw * 4096 + j * 1024, vX[j], rX, s * 256);
    };
    auto issueW = [&](u32 base, int s, u32 col) {
#pragma unroll
      for (int n = 0; n < 6; n++) dma<0>(base + 16384 + (lw * 6 + n) * 1024, vW[n], rW, s * (128 * 2752) + col);
    };
    u32x2 ep = ld8_issue(WS_EPOCH, rWS);
    if (lw == 0) dma<0>(sb + L_GW, lane * 16, mkrsrc(gw, 1024), 0);
    issueX(sb, 0);
    issueW(sb, 0, 2 * FH);
    issueX(sb + SLOT, 1);
    issueW(sb + SLOT, 1, 2 * FH);
    WAITV1(10, ep);
    const u64 tag = (((u64)__builtin_amdgcn_readfirstlane(ep[1]) << 32) | (u32)__builtin_amdgcn_readfirstlane(ep[0])) * 6364136223846793005ull + 1442695040888963407ull;
    const u32 tlo = (u32)tag, thi = (u32)(tag >> 32);

    const i32x4 rB = mkrsrc(lw >> 1 ? bim : bre, 512 * 512 * 2);
    u32 vB[8];
#pragma unroll
    for (int j = 0; j < 8; j++) {
      int ch = 32 * (lw & 1) + 4 * j + g;
      vB[j] = (64 * j2 + ch) * 1024 + ((m ^ (ch & 15)) << 4);
    }
    auto issueB = [&](u32 base, int s) {
#pragma unroll
      for (int j = 0; j < 8; j++) dma<0>(base + lw * 8192 + j * 1024, vB[j], rB, s * 256);
    };

    f32x4 gacc = {0.f, 0.f, 0.f, 0.f};
#pragma unroll
    for (int s = 0; s < 4; s++) {
      LBAR();
      STAMP(s);
      const u32 nb = sb + ((s + 2) % 3) * SLOT;
      if (s < 2) { issueX(nb, s + 2); issueW(nb, s + 2, 2 * FH); } else issueW(nb, s - 2, 0);
#pragma unroll
      for (int ks = 0; ks < 4; ks++)
        gacc = MFMA(lds16(sb + (s % 3) * SLOT + (16 * lw + m) * 256 + (((4 * ks + g) ^ m) << 4)), lds16(sb + L_GW + s * 256 + ks * 64 + g * 16), gacc);
      if (s < 2) WAITV(10); else WAITV(6);
    }
    {
      f32x4 gt;
#pragma unroll
      for (int r = 0; r < 4; r++) gt[r] = 1.0f / (1.0f + expf(-gacc[r]));
      if (m == 0) *(LAS f32x4*)(size_t)(sb + L_GATE + (16 * lw + 4 * g) * 4) = gt;
    }
    LBAR(); STAMP(4); issueW(sb + 0 * SLOT, 2, 0); WAITV(6);
    LBAR(); STAMP(5); issueW(sb + 1 * SLOT, 3, 0); WAITV(6);
    LBAR(); STAMP(6); issueB(sb + 2 * SLOT, 0); WAITV(8);
    LBAR(); STAMP(7);
    {
      const u32 vs = WS_STAT + ((i2 * 16 + 4 * g) * 64 + 16 * lw + m) * 16;
      u32x4 p0 = ld16_issue(vs, rWS), p1 = ld16_issue(vs + 1024, rWS), p2 = ld16_issue(vs + 2048, rWS), p3 = ld16_issue(vs + 3072, rWS);
      issueB(sb + 0 * SLOT, 1);
      WAITV4(8, p0, p1, p2, p3);
      const u64 t0 = __builtin_amdgcn_s_memrealtime();
      while (!__all(p0[1] == tlo && p0[3] == thi && p1[1] == tlo && p1[3] == thi && p2[1] == tlo && p2[3] == thi && p3[1] == tlo && p3[3] == thi)) {
        if (__builtin_amdgcn_s_memrealtime() - t0 > SPIN_TICKS) break;
        __builtin_amdgcn_s_sleep(1);
        p0 = ld16_issue(vs, rWS); p1 = ld16_issue(vs + 1024, rWS); p2 = ld16_issue(vs + 2048, rWS); p3 = ld16_issue(vs + 3072, rWS);
        WAITV4(0, p0, p1, p2, p3);
      }
      float s1 = (__uint_as_float(p0[0]) + __uint_as_float(p1[0])) + (__uint_as_float(p2[0]) + __uint_as_float(p3[0]));
      float s2 = (__uint_as_float(p0[2]) + __uint_as_float(p1[2])) + (__uint_as_float(p2[2]) + __uint_as_float(p3[2]));
      s1 += __shfl_xor(s1, 16); s2 += __shfl_xor(s2, 16);
      s1 += __shfl_xor(s1, 32); s2 += __shfl_xor(s2, 32);
      const float mean = s1 * (1.0f / FH), var = fmaxf(s2 * (1.0f / FH) - mean * mean, 0.f);
      const float rstd = 1.0f / sqrtf(var + 1e-5f);
      if (g == 0) *(LAS f32x2*)(size_t)(sb + L_STAT + (16 * lw + m) * 8) = f32x2{rstd, -mean * rstd};
    }
    const i32x4 rA = mkrsrc(wsp, 2048 * 2048 * 2);
    u32 vA[4];
#pragma unroll
    for (int j = 0; j < 4; j++) vA[j] = (64 * i2 + 32 * (lw >> 1) + 8 * j + 4 * (lw & 1) + g) * 4096 + ((m ^ (4 * j + g)) << 4);
    const u32 vZ = WS_Z + (lw * 48 + 6 * j2) * 1024 + lane * 16;
    auto issueA = [&](u32 base, int s) {
#pragma unroll
      for (int j = 0; j < 4; j++) dma<0>(base + lw * 4096 + j * 1024, vA[j], rA, s * 256);
    };
    auto issueZ = [&](u32 base, int s) {
#pragma unroll
      for (int n = 0; n < 6; n++) dma<1>(base + 16384 + (lw * 6 + n) * 1024, vZ, rWS, s * (4 * 48 * 1024) + n * 1024);
    };
    LBAR(); STAMP(8); issueB(sb + 1 * SLOT, 2); WAITV(8);
    LBAR(); STAMP(9); issueB(sb + 2 * SLOT, 3); WAITV(8);
    LBAR(); STAMP(10); issueA(sb + 0 * SLOT, 0); WAITV(4);
    LBAR(); STAMP(11);
    {
      const u32 vf = WS_F1 + j2 * 1024 + lane * 16;
      u32x4 f = ld16_issue(vf, rWS);
      issueA(sb + 1 * SLOT, 1);
      WAITV1(4, f);
      if (!flags_ok(f, tlo, thi, true)) spin_flags(vf, rWS, tlo, thi, true);
      STAMP(48);
      issueZ(sb + 0 * SLOT, 0);
      issueZ(sb + 1 * SLOT, 1);
      if (blockIdx.x == 0 && lw == 0 && lane == 0) __hip_atomic_store((GAS u64*)(ws + WS_EPOCH), tag, __ATOMIC_RELAXED, __HIP_MEMORY_SCOPE_AGENT);
      if (blockIdx.x == 0 && lw == 0) WAITV(7); else WAITV(6);
    }
    u32 nb = sb + 2 * SLOT;
    auto rot = [&]() { nb += SLOT; if (nb == sb + 3 * SLOT) nb = sb; };
    for (int s = 0; s < 3; s++) {
      LBAR(); STAMP(12 + s); issueZ(nb, s + 2); issueA(nb, s + 2); rot(); WAITV(10);
    }
    {
      LBAR(); STAMP(15);
      const u32 vf = WS_FW + j2 * 1024 + lane * 16;
      const bool need = (lane >> 1) <= i2;
      u32x4 f = ld16_issue(vf, rWS);
      issueZ(nb, 5); issueA(nb, 5); rot();
      WAITV1(10, f);
      if (!flags_ok(f, tlo, thi, need)) spin_flags(vf, rWS, tlo, thi, need);
      LBAR(); STAMP(16);
      u32x2 w[17];
      const u32 vs = WS_SEG + j2 * (64 * 512) + (16 * lw + m) * 8;
#pragma unroll
      for (int i = 0; i < 16; i++) {
        int sg = 2 * i2 - 64 + 16 * g + i;
        w[i] = ld8_issue(sg >= 0 ? vs + sg * 512 : 0x80000000u, rWS);
      }
      w[16] = ld8_issue(vs + 2 * i2 * 512, rWS);
      issueZ(nb, 6); issueA(nb, 6); rot();
      asm volatile("s_waitcnt vmcnt(10)"
                   : "+v"(w[0]), "+v"(w[1]), "+v"(w[2]), "+v"(w[3]), "+v"(w[4]), "+v"(w[5]), "+v"(w[6]), "+v"(w[7]), "+v"(w[8]), "+v"(w[9]),
                     "+v"(w[10]), "+v"(w[11]), "+v"(w[12]), "+v"(w[13]), "+v"(w[14]), "+v"(w[15]), "+v"(w[16])::"memory");
      LBAR(); STAMP(17);
      issueZ(nb, 7); issueA(nb, 7); rot();
      const f32x4 pw = *(const LAS f32x4*)(size_t)(sb + L_POW + 2048 + (16 * lw + m) * 16);
      const cf l32 = {pw[0], pw[1]}, l512 = {pw[2], pw[3]};
      cf h = {__uint_as_float(w[0][0]), __uint_as_float(w[0][1])};
#pragma unroll
      for (int i = 1; i < 16; i++) h = cfma(l32, h, cf{__uint_as_float(w[i][0]), __uint_as_float(w[i][1])});
      cf c0 = {__shfl(h.re, m), __shfl(h.im, m)};
#pragma unroll
      for (int k = 1; k < 4; k++) c0 = cfma(l512, c0, cf{__shfl(h.re, m + 16 * k), __shfl(h.im, m + 16 * k)});
      cf c1 = cfma(l32, c0, cf{__uint_as_float(w[16][0]), __uint_as_float(w[16][1])});
      if (g < 2) *(LAS f32x2*)(size_t)(sb + L_CARRY + g * 512 + (16 * lw + m) * 8) = g ? f32x2{c1.re, c1.im} : f32x2{c0.re, c0.im};
      WAITV(10);
    }
    for (int s = 6; s < 14; s++) {
      LBAR(); STAMP(12 + s); issueZ(nb, s + 2); issueA(nb, s + 2); rot(); WAITV(10);
    }
    const i32x4 rC0 = mkrsrc(cre, 512 * 512 * 2), rC1 = mkrsrc(cim, 512 * 512 * 2);
    u32 vC[4];
#pragma unroll
    for (int j = 0; j < 4; j++) vC[j] = (64 * j2 + 16 * lw + 4 * j + g) * 1024 + ((m ^ (4 * j + g)) << 4);
    const u32 vH = WS_H + ((2 * i2 + (lw >> 1)) * 32 + 4 * (lw & 1)) * 1024 + lane * 16;
    auto issueC = [&](u32 base, int s) {
#pragma unroll
      for (int j = 0; j < 4; j++) {
        if (s < 4) dma<0>(base + 16384 + lw * 4096 + j * 1024, vC[j], rC0, s * 256);
        else dma<0>(base + 16384 + lw * 4096 + j * 1024, vC[j], rC1, (s - 4) * 256);
      }
    };
    auto issueH = [&](u32 base, int s) {
#pragma unroll
      for (int j = 0; j < 4; j++) dma<1>(base + lw * 4096 + j * 1024, vH, rWS, (s >> 2) * (64 * 32 * 1024) + (s & 3) * 8192 + j * 1024);
    };
    {
      LBAR(); STAMP(26);
      const u32 vf = WS_F2 + i2 * 256 + m * 16;
      u32x4 f = ld16_issue(vf, rWS);
      issueC(nb, 0);
      WAITV1(4, f);
      if (!flags_ok(f, tlo, thi, true)) spin_flags(vf, rWS, tlo, thi, true);
      issueH(nb, 0); rot();
      WAITV(8);
    }
    for (int s = 1; s < 4; s++) {
      LBAR(); STAMP(26 + s); issueH(nb, s); issueC(nb, s); rot(); WAITV(8);
    }
    for (int s = 4; s < 8; s++) {
      LBAR(); STAMP(26 + s); issueH(nb, s); issueC(nb, s); rot(); WAITV(8);
    }
    const i32x4 rO = mkrsrc(wout, FH * 512 * 2);
    u32 vO[4];
#pragma unroll
    for (int j = 0; j < 4; j++) {
      int row = 32 * lw + 8 * j + (lane >> 3);
      vO[j] = row * 1024 + j2 * 128 + (((lane & 7) ^ (((row >> 1) & 3) << 1)) << 4);
    }
    const u32 vV = WS_V + ((2 * i2 + (lw >> 1)) * 48 + 4 * (lw & 1)) * 1024 + lane * 16;
    auto issueO = [&](u32 base, int s) {
#pragma unroll
      for (int j = 0; j < 4; j++) dma<0>(base + 16384 + lw * 4096 + j * 1024, vO[j], rO, s * (128 * 1024));
    };
    auto issueV = [&](u32 base, int s) {
#pragma unroll
      for (int j = 0; j < 4; j++) dma<1>(base + lw * 4096 + j * 1024, vV, rWS, s * 8192 + j * 1024);
    };
    {
      LBAR(); STAMP(34);
      const u32 vf = WS_F3 + i2 * 256 + m * 16;
      u32x4 f = ld16_issue(vf, rWS);
      issueO(nb, 0);
      WAITV1(4, f);
      if (!flags_ok(f, tlo, thi, true)) spin_flags(vf, rWS, tlo, thi, true);
      STAMP(49);
      issueV(nb, 0); rot();
      WAITV(8);
    }
    for (int s = 1; s < 5; s++) {
      LBAR(); STAMP(34 + s); issueV(nb, s); issueO(nb, s); rot(); WAITV(8);
    }
    LBAR(); STAMP(39);
    if ((lw & 1) == 0) issueV(nb, 5);
    if (lw < 2) issueO(nb, 5);
    if (lw == 0) WAITV(8); else if (lw == 3) WAITV(0); else WAITV(4);
    LBAR(); STAMP(40);
    WAITV(0);
    LBAR(); STAMP(41);
  } else {
    const int wr = wave >> 1, wc = wave & 1, qq = m >> 2, p = lane & 3;
    const __amdgpu_buffer_rsrc_t rS = __builtin_amdgcn_make_buffer_rsrc(ws, 0, WS_END, 0x00020000);
    const u64 ep = __hip_atomic_load((GAS u64*)(ws + WS_EPOCH), __ATOMIC_RELAXED, __HIP_MEMORY_SCOPE_AGENT);
    const int trow = 64 * i2 + 32 * wr + 8 * (m >> 2) + (m & 3);
    const float bias0 = bf2f(bsp[trow]), bias1 = bf2f(bsp[trow + 4]);
    if (wave == 0) {
      cf lam, coef;
      pole(lre, lim, ldt, 64 * j2 + lane, lam, coef);
      cf t = cmul(lam, lam);
      t = cmul(t, t);
      const cf l8 = cmul(t, t), l16 = cmul(l8, l8), l32 = cmul(l16, l16);
      t = cmul(l32, l32);
      t = cmul(t, t);
      t = cmul(t, t);
      t = cmul(t, t);
      *(LAS f32x4*)(size_t)(sb + L_POW + lane * 16) = f32x4{lam.re, lam.im, 2.f * coef.re, 2.f * coef.im};
      *(LAS f32x4*)(size_t)(sb + L_POW + 1024 + lane * 16) = f32x4{l8.re, l8.im, l16.re, l16.im};
      *(LAS f32x4*)(size_t)(sb + L_POW + 2048 + lane * 16) = f32x4{l32.re, l32.im, t.re, t.im};
    }
    const u64 tag = (((u64)__builtin_amdgcn_readfirstlane((u32)(ep >> 32)) << 32) | (u32)__builtin_amdgcn_readfirstlane((u32)ep)) * 6364136223846793005ull + 1442695040888963407ull;
    const u32 tlo = (u32)tag, thi = (u32)(tag >> 32);
    const int tile0 = 6 * j2 + 3 * wc;
    const int rg = 2 * i2 + wr;

    u32 aR[4], aW[3];
#pragma unroll
    for (int ks = 0; ks < 4; ks++) aR[ks] = sb + m * 256 + (((4 * ks + g) ^ m) << 4);
#pragma unroll
    for (int n = 0; n < 3; n++) aW[n] = sb + 16384 + (8 * g + qq) * 192 + (((3 * wc + n) ^ (g & 1)) << 5) + p * 8;

    bf16x8 xf[2][16];
    bf16x8 wf[2][3];
    auto rdW = [&](bf16x8* w, int kk, int st) {
      const u32 o = (st % 3) * SLOT + (kk & 3) * 6144;
#pragma unroll
      for (int n = 0; n < 3; n++) w[n] = cat(ldstr(aW[n] + o), ldstr(aW[n] + o + 768));
    };
    auto rdX = [&](int kk) {
#pragma unroll
      for (int u = 0; u < 2; u++) xf[u][kk] = lds16(aR[kk & 3] + ((kk >> 2) % 3) * SLOT + (32 * wr + 16 * u) * 256);
    };

    f32x4 z2[2][3] = {};
    __builtin_amdgcn_s_waitcnt(0xC07F);
    LBAR();
    rdX(0); rdW(wf[0], 0, 0);
#pragma unroll
    for (int kk = 0; kk < 16; kk++) {
      if (kk < 15) {
        if ((kk & 3) == 3) LBAR();
        rdX(kk + 1); rdW(wf[(kk + 1) & 1], kk + 1, (kk + 1) >> 2);
      }
      SB();
#pragma unroll
      for (int u = 0; u < 2; u++)
#pragma unroll
        for (int n = 0; n < 3; n++) z2[u][n] = MFMA(xf[u][kk], wf[kk & 1][n], z2[u][n]);
      SB();
    }
    STAMP(0);
    u32 z2p[3][4];
    {
      float s1[8], s2[8];
#pragma unroll
      for (int j = 0; j < 8; j++) { s1[j] = 0.f; s2[j] = 0.f; }
#pragma unroll
      for (int n = 0; n < 3; n++) {
        const bool ok = tile0 + n < FH / 16;
#pragma unroll
        for (int j = 0; j < 8; j++) {
          float v = ok ? gelu(z2[j >> 2][n][j & 3]) : 0.f;
          z2[j >> 2][n][j & 3] = v;
          s1[j] += v;
          s2[j] = __builtin_fmaf(v, v, s2[j]);
        }
#pragma unroll
        for (int k = 0; k < 4; k++) z2p[n][k] = pk2(z2[k >> 1][n][(2 * k) & 3], z2[k >> 1][n][(2 * k + 1) & 3]);
      }
      const float r1 = reduce8(s1, m), r2 = reduce8(s2, m);
      if (!(m & 1))
        __builtin_amdgcn_raw_buffer_store_b128(u32x4{__float_as_uint(r1), tlo, __float_as_uint(r2), thi}, rS,
                                               WS_STAT + ((i2 * 16 + 2 * j2 + wc) * 64 + 32 * wr + 8 * g + (m >> 1)) * 16, 0, 16);
    }
    STAMP(1);
    u32 z1p[2][3][2];
    {
      f32x4 z1[2][3] = {};
      LBAR();
      rdW(wf[0], 0, 4);
#pragma unroll
      for (int kk = 0; kk < 16; kk++) {
        if (kk < 15) {
          if ((kk & 3) == 3) LBAR();
          rdW(wf[(kk + 1) & 1], kk + 1, 4 + ((kk + 1) >> 2));
        }
        SB();
#pragma unroll
        for (int u = 0; u < 2; u++)
#pragma unroll
          for (int n = 0; n < 3; n++) z1[u][n] = MFMA(wf[kk & 1][n], xf[u][kk], z1[u][n]);
        SB();
      }
      STAMP(2);
#pragma unroll
      for (int u = 0; u < 2; u++)
#pragma unroll
        for (int n = 0; n < 3; n++) {
          z1p[u][n][0] = pk2(gelu(z1[u][n][0]), gelu(z1[u][n][1]));
          z1p[u][n][1] = pk2(gelu(z1[u][n][2]), gelu(z1[u][n][3]));
        }
    }
    STAMP(3);
    LBAR();
    STAMP(4);
    {
      const u32 as = sb + L_STAT + (32 * wr + 8 * g) * 8;
      f32x4 st[4];
#pragma unroll
      for (int k = 0; k < 4; k++) st[k] = *(const LAS f32x4*)(size_t)(as + k * 16);
#pragma unroll
      for (int n = 0; n < 3; n++) {
        const bool ok = tile0 + n < FH / 16;
        u32x4 o;
#pragma unroll
        for (int k = 0; k < 4; k++) {
          float a = __builtin_fmaf(lo16(z2p[n][k]), st[k][0], st[k][1]);
          float b = __builtin_fmaf(hi16(z2p[n][k]), st[k][2], st[k][3]);
          o[k] = ok ? pk2(a, b) : 0u;
        }
        __builtin_amdgcn_raw_buffer_store_b128(o, rS, WS_Z + (rg * 48 + tile0 + n) * 1024 + lane * 16, 0, 16);
      }
    }
    STAMP(5);
    f32x4 ur[2][2] = {}, ui[2][2] = {};
    {
      bf16x8 bf[2][4];
      auto rdB = [&](bf16x8* b, int kk) {
        const u32 o = ((8 + (kk >> 2)) % 3) * SLOT + 32 * wc * 256;
#pragma unroll
        for (int c = 0; c < 4; c++) b[c] = lds16(aR[kk & 3] + o + (c >> 1) * 16384 + (c & 1) * 4096);
      };
      rdB(bf[0], 0);
#pragma unroll
      for (int kk = 0; kk < 16; kk++) {
        if (kk < 15) {
          if ((kk & 3) == 3) LBAR();
          rdB(bf[(kk + 1) & 1], kk + 1);
        }
        SB();
#pragma unroll
        for (int u = 0; u < 2; u++)
#pragma unroll
          for (int a = 0; a < 2; a++) {
            ur[u][a] = MFMA(xf[u][kk], bf[kk & 1][a], ur[u][a]);
            ui[u][a] = MFMA(xf[u][kk], bf[kk & 1][2 + a], ui[u][a]);
          }
        SB();
        if (kk == 2) put_flag(ws, WS_F1 + ((j2 * 32 + i2) * 4 + wave) * 8, tag, lane);
      }
    }
    STAMP(6);
    cf pre[2];
#pragma unroll
    for (int a = 0; a < 2; a++) {
      const u32 ap = sb + L_POW + (32 * wc + 16 * a + m) * 16;
      const f32x4 t0 = *(const LAS f32x4*)(size_t)ap, t1 = *(const LAS f32x4*)(size_t)(ap + 1024);
      const cf lam = {t0[0], t0[1]}, l8 = {t1[0], t1[1]}, l16 = {t1[2], t1[3]};
      cf e = {ur[0][a][0], ui[0][a][0]};
#pragma unroll
      for (int j = 1; j < 8; j++) e = cfma(lam, e, cf{ur[j >> 2][a][j & 3], ui[j >> 2][a][j & 3]});
      cf o = {__shfl_up(e.re, 16), __shfl_up(e.im, 16)};
      if (g >= 1) e = cfma(l8, o, e);
      o = {__shfl_up(e.re, 32), __shfl_up(e.im, 32)};
      if (g >= 2) e = cfma(l16, o, e);
      o = {__shfl_up(e.re, 16), __shfl_up(e.im, 16)};
      pre[a] = g ? o : cf{0.f, 0.f};
      if (g == 3)
        __builtin_amdgcn_raw_buffer_store_b64(u32x2{__float_as_uint(e.re), __float_as_uint(e.im)}, rS,
                                              WS_SEG + (j2 * 64 + rg) * 512 + (32 * wc + 16 * a + m) * 8, 0, 16);
    }
    STAMP(7);
    f32x4 ga[2][3] = {};
    {
      const u32 aZ = sb + 16384 + 3 * wc * 1024 + lane * 16;
      bf16x8 ca[3], cb[2], na[3], nbf[2];
      auto rd = [&](bf16x8* a, bf16x8* b, u32 so, int ks) {
        b[0] = lds16(aR[ks] + so + 32 * wr * 256);
#pragma unroll
        for (int n = 0; n < 3; n++) a[n] = lds16(aZ + so + (ks * 6 + n) * 1024);
        b[1] = lds16(aR[ks] + so + (32 * wr + 16) * 256);
      };
      auto mm = [&](bf16x8* a, bf16x8* b) {
#pragma unroll
        for (int u = 0; u < 2; u++)
#pragma unroll
          for (int n = 0; n < 3; n++) ga[u][n] = MFMA(a[n], b[u], ga[u][n]);
      };
      u32 so = 0;
      LBAR();
      STAMP(8);
      rd(ca, cb, so, 0);
      for (int s = 0; s < 16; s++) {
        rd(na, nbf, so, 1); SB(); mm(ca, cb); SB();
        rd(ca, cb, so, 2); SB(); mm(na, nbf); SB();
        rd(na, nbf, so, 3); SB(); mm(ca, cb); SB();
        if (s == 0) put_flag(ws, WS_FW + ((j2 * 32 + i2) * 4 + wave) * 8, tag, lane);
        if (s == 9) put_flag(ws, WS_F2 + ((i2 * 8 + j2) * 4 + wave) * 8, tag, lane);
        if (s == 8) {
#pragma unroll
          for (int a = 0; a < 2; a++) {
            const f32x2 cw = *(const LAS f32x2*)(size_t)(sb + L_CARRY + wr * 512 + (32 * wc + 16 * a + m) * 8);
            const u32 ap = sb + L_POW + (32 * wc + 16 * a + m) * 16;
            const f32x4 t0 = *(const LAS f32x4*)(size_t)ap, t1 = *(const LAS f32x4*)(size_t)(ap + 1024);
            const cf lam = {t0[0], t0[1]}, c2 = {t0[2], t0[3]};
            cf lq = (g & 1) ? cf{t1[0], t1[1]} : cf{1.f, 0.f};
            if (g & 2) lq = cmul(lq, cf{t1[2], t1[3]});
            cf h = cfma(lq, cf{cw[0], cw[1]}, pre[a]);
            float hr[8], hi[8];
#pragma unroll
            for (int j = 0; j < 8; j++) {
              h = cfma(lam, h, cf{ur[j >> 2][a][j & 3], ui[j >> 2][a][j & 3]});
              hr[j] = c2.re * h.re - c2.im * h.im;
              hi[j] = -(c2.re * h.im + c2.im * h.re);
            }
            const bool sw = m & 8;
            const u32 r0 = pk2(hr[0], hr[1]), r1 = pk2(hr[2], hr[3]), r2 = pk2(hr[4], hr[5]), r3 = pk2(hr[6], hr[7]);
            const u32 i0 = pk2(hi[0], hi[1]), i1 = pk2(hi[2], hi[3]), i2_ = pk2(hi[4], hi[5]), i3 = pk2(hi[6], hi[7]);
            const u32 off = WS_H + (rg * 32 + 4 * j2 + 2 * wc + a) * 1024 + (m * 4 + g) * 16;
            __builtin_amdgcn_raw_buffer_store_b128(sw ? u32x4{r2, r3, r0, r1} : u32x4{r0, r1, r2, r3}, rS, off, 0, 16);
            __builtin_amdgcn_raw_buffer_store_b128(sw ? u32x4{i2_, i3, i0, i1} : u32x4{i0, i1, i2_, i3}, rS, off + 64 * 32 * 1024, 0, 16);
          }
        }
        so += SLOT;
        if (so == 3 * SLOT) so = 0;
        if (s < 15) { LBAR(); rd(ca, cb, so, 0); }
        SB(); mm(na, nbf); SB();
      }
    }
    STAMP(9);
#pragma unroll
    for (int n = 0; n < 3; n++) {
      if (tile0 + n < 44) {
        const bool ok = tile0 + n < FH / 16;
        u32 h[2][2];
#pragma unroll
        for (int u = 0; u < 2; u++) {
          const float b = u ? bias1 : bias0;
          h[u][0] = pk2(lo16(z1p[u][n][0]) * (ga[u][n][0] + b), hi16(z1p[u][n][0]) * (ga[u][n][1] + b));
          h[u][1] = pk2(lo16(z1p[u][n][1]) * (ga[u][n][2] + b), hi16(z1p[u][n][1]) * (ga[u][n][3] + b));
        }
        u32x4 o = (g & 1) ? u32x4{h[1][0], h[1][1], h[0][0], h[0][1]} : u32x4{h[0][0], h[0][1], h[1][0], h[1][1]};
        if (!ok) o = u32x4{0u, 0u, 0u, 0u};
        __builtin_amdgcn_raw_buffer_store_b128(o, rS, WS_V + (rg * 48 + tile0 + n) * 1024 + lane * 16, 0, 16);
      }
    }
    STAMP(10);
    f32x4 ya[2][2] = {}, yb[2][2] = {};
    const int dcol = 64 * j2 + 32 * wc + 4 * g;
    u32x2 xe[2][2], de[2];
    {
      u32 aH[2];
#pragma unroll
      for (int u = 0; u < 2; u++) aH[u] = sb + (wr * 8 + (g >> 1)) * 1024 + (8 * (g & 1) + qq) * 64 + p * 16 + ((u ^ (g & 1)) << 3);
      bf16x8 ca[2], cb[2], na[2], nbf[2];
      auto rd = [&](bf16x8* a, bf16x8* b, u32 so, int ks) {
#pragma unroll
        for (int u = 0; u < 2; u++) {
          b[u] = cat(ldstr(aH[u] + so + ks * 2048), ldstr(aH[u] + so + ks * 2048 + 256));
          a[u] = lds16(aR[ks] + so + 16384 + (32 * wc + 16 * u) * 256);
        }
      };
      auto mm = [&](bf16x8* a, bf16x8* b) {
#pragma unroll
        for (int u = 0; u < 2; u++)
#pragma unroll
          for (int n = 0; n < 2; n++) ya[u][n] = MFMA(a[n], b[u], ya[u][n]);
      };
      u32 so = SLOT;
      LBAR();
      rd(ca, cb, so, 0);
      for (int s = 0; s < 8; s++) {
        rd(na, nbf, so, 1); SB(); mm(ca, cb); SB();
        rd(ca, cb, so, 2); SB(); mm(na, nbf); SB();
        rd(na, nbf, so, 3); SB(); mm(ca, cb); SB();
        if (s == 0) put_flag(ws, WS_F3 + ((i2 * 8 + j2) * 4 + wave) * 8, tag, lane);
        so += SLOT;
        if (so == 3 * SLOT) so = 0;
        if (s < 7) { LBAR(); rd(ca, cb, so, 0); }
        SB(); mm(na, nbf); SB();
      }
    }
    STAMP(11);
#pragma unroll
    for (int n = 0; n < 2; n++) {
      de[n] = *(const u32x2*)(dsk + dcol + 16 * n);
#pragma unroll
      for (int u = 0; u < 2; u++) xe[u][n] = *(const u32x2*)(x + (trow + 4 * u) * 512 + dcol + 16 * n);
    }
    {
      u32 aV[2], aO[2];
#pragma unroll
      for (int u = 0; u < 2; u++) {
        aV[u] = sb + wr * 8192 + lane * 16 + ((u ^ (g & 1)) << 3);
        aO[u] = sb + 16384 + (4 * g + qq) * 128 + (((2 * wc + u) ^ (2 * (g & 1) + (qq >> 1))) << 5) + p * 8;
      }
      bf16x8 ca[2], cb[2], na[2], nbf[2];
      auto rd = [&](bf16x8* a, bf16x8* b, u32 so, int ks) {
#pragma unroll
        for (int u = 0; u < 2; u++) {
          b[u] = cat(lds8(aV[u] + so + ks * 2048), lds8(aV[u] + so + ks * 2048 + 1024));
          a[u] = cat(ldstr(aO[u] + so + ks * 4096), ldstr(aO[u] + so + ks * 4096 + 2048));
        }
      };
      auto mm = [&](bf16x8* a, bf16x8* b) {
#pragma unroll
        for (int u = 0; u < 2; u++)
#pragma unroll
          for (int n = 0; n < 2; n++) yb[u][n] = MFMA(a[n], b[u], yb[u][n]);
      };
      u32 so = 0;
      LBAR();
      rd(ca, cb, so, 0);
      for (int s = 0; s < 5; s++) {
        rd(na, nbf, so, 1); SB(); mm(ca, cb); SB();
        rd(ca, cb, so, 2); SB(); mm(na, nbf); SB();
        rd(na, nbf, so, 3); SB(); mm(ca, cb); SB();
        so += SLOT;
        if (so == 3 * SLOT) so = 0;
        LBAR(); rd(ca, cb, so, 0);
        SB(); mm(na, nbf); SB();
      }
      rd(na, nbf, so, 1); SB(); mm(ca, cb); SB();
      mm(na, nbf);
    }
    STAMP(12);
#pragma unroll
    for (int u = 0; u < 2; u++) {
      const float gt = *(const LAS float*)(size_t)(sb + L_GATE + (32 * wr + 16 * u + m) * 4);
#pragma unroll
      for (int n = 0; n < 2; n++) {
        f32x4 o;
        o[0] = __builtin_fmaf(lo16(xe[u][n][0]), lo16(de[n][0]), ya[u][n][0]);
        o[1] = __builtin_fmaf(hi16(xe[u][n][0]), hi16(de[n][0]), ya[u][n][1]);
        o[2] = __builtin_fmaf(lo16(xe[u][n][1]), lo16(de[n][1]), ya[u][n][2]);
        o[3] = __builtin_fmaf(hi16(xe[u][n][1]), hi16(de[n][1]), ya[u][n][3]);
#pragma unroll
        for (int r = 0; r < 4; r++) o[r] = gt * o[r] + (1.0f - gt) * yb[u][n][r];
        *(f32x4*)(out + (trow + 4 * u) * 512 + dcol + 16 * n) = o;
      }
    }
    STAMP(13);
  }
}
}

extern "C" void kernel_launch(void* const* d_in, const int* in_sizes, int n_in, void* d_out, int out_size, void* d_ws, size_t ws_size,
                              hipStream_t stream) {
  (void)hipFuncSetAttribute((const void*)fused_kernel, hipFuncAttributeMaxDynamicSharedMemorySize, LDS_BYTES);
  fused_kernel<<<256, 512, LDS_BYTES, stream>>>((const u16*)d_in[0], (const u16*)d_in[1], (const u16*)d_in[2], (const u16*)d_in[3],
                                                (const u16*)d_in[4], (const u16*)d_in[5], (const u16*)d_in[6], (const u16*)d_in[7],
                                                (const u16*)d_in[8], (const u16*)d_in[10], (const u16*)d_in[11], (const u16*)d_in[12],
                                                (const u16*)d_in[13], (const u16*)d_in[14], (float*)d_out, (char*)d_ws);
}
```

```cpp
#define AMPL 0
#include <hip/hip_runtime.h>
#include <stdint.h>

namespace {
typedef unsigned long long u64;
typedef unsigned short u16;
typedef unsigned u32;
typedef short bf16x8 __attribute__((ext_vector_type(8)));
typedef short s16x4 __attribute__((ext_vector_type(4)));
typedef float f32x4 __attribute__((ext_vector_type(4)));
typedef float f32x2 __attribute__((ext_vector_type(2)));
typedef __bf16 bf2 __attribute__((ext_vector_type(2)));
typedef int i32x4 __attribute__((ext_vector_type(4)));
typedef u32 u32x4 __attribute__((ext_vector_type(4)));
typedef u32 u32x2 __attribute__((ext_vector_type(2)));
#define LAS __attribute__((address_space(3)))
#define GAS __attribute__((address_space(1)))
#define DI __device__ __forceinline__

constexpr int FH = 688;
constexpr u32 SLOT = 40960;
constexpr u32 L_GW = 3 * SLOT, L_STAT = L_GW + 1024, L_GATE = L_STAT + 512, L_CARRY = L_GATE + 256;
constexpr u32 L_POW = L_CARRY + 1024;
constexpr u32 LDS_BYTES = L_POW + 3072;

constexpr u32 WS_EPOCH = 0;
constexpr u32 WS_F1 = 4096, WS_FW = WS_F1 + 8192, WS_F2 = WS_FW + 8192, WS_F3 = WS_F2 + 8192;
constexpr u32 WS_STAT = 65536;
constexpr u32 WS_SEG = WS_STAT + 32 * 16 * 64 * 16;
constexpr u32 WS_Z = 1u << 20;
constexpr u32 WS_H = WS_Z + 64 * 48 * 1024;
constexpr u32 WS_V = WS_H + 2 * 64 * 32 * 1024;
constexpr u32 WS_END = WS_V + 64 * 48 * 1024;
constexpr u32 WS_STAMP = 16u << 20;
constexpr u64 SPIN_TICKS = 2000000;

DI i32x4 mkrsrc(const void* p, u32 bytes) {
  u64 a = (u64)p;
  i32x4 r;
  r[0] = __builtin_amdgcn_readfirstlane((u32)a);
  r[1] = __builtin_amdgcn_readfirstlane((u32)(a >> 32) & 0xffff);
  r[2] = bytes;
  r[3] = 0x00020000;
  return r;
}
template <int SC1>
DI void dma(u32 lds, u32 voff, i32x4 rs, u32 soff) {
  if constexpr (SC1)
    asm volatile("s_mov_b32 m0, %0\n\ts_nop 0\n\tbuffer_load_dwordx4 %1, %2, %3 offen sc1 lds" ::"s"(lds), "v"(voff), "s"(rs), "s"(soff) : "memory");
  else
    asm volatile("s_mov_b32 m0, %0\n\ts_nop 0\n\tbuffer_load_dwordx4 %1, %2, %3 offen lds" ::"s"(lds), "v"(voff), "s"(rs), "s"(soff) : "memory");
}
DI u32x4 ld16_issue(u32 voff, i32x4 rs) {
  u32x4 r;
  asm volatile("buffer_load_dwordx4 %0, %1, %2, 0 offen sc1" : "=v"(r) : "v"(voff), "s"(rs) : "memory");
  return r;
}
DI u32x2 ld8_issue(u32 voff, i32x4 rs) {
  u32x2 r;
  asm volatile("buffer_load_dwordx2 %0, %1, %2, 0 offen sc1" : "=v"(r) : "v"(voff), "s"(rs) : "memory");
  return r;
}
#define WAITV(n) asm volatile("s_waitcnt vmcnt(%0)" ::"n"(n) : "memory")
#define WAITV1(n, a) asm volatile("s_waitcnt vmcnt(%1)" : "+v"(a) : "n"(n) : "memory")
#define WAITV4(n, a, b, c, d) asm volatile("s_waitcnt vmcnt(%4)" : "+v"(a), "+v"(b), "+v"(c), "+v"(d) : "n"(n) : "memory")
#define LBAR() asm volatile("s_waitcnt lgkmcnt(0)\n\ts_barrier" ::: "memory")
#define SB() __builtin_amdgcn_sched_barrier(0)
#define MFMA(a, b, c) __builtin_amdgcn_mfma_f32_16x16x32_bf16((a), (b), (c), 0, 0, 0)

DI bf16x8 lds16(u32 a) { return *(const LAS bf16x8*)(size_t)a; }
DI s16x4 lds8(u32 a) { return *(const LAS s16x4*)(size_t)a; }
DI s16x4 ldstr(u32 a) { return __builtin_amdgcn_ds_read_tr16_b64_v4i16((LAS s16x4*)(size_t)a); }
DI bf16x8 cat(s16x4 lo, s16x4 hi) { return __builtin_shufflevector(lo, hi, 0, 1, 2, 3, 4, 5, 6, 7); }
DI float bf2f(u16 v) { return __uint_as_float((u32)v << 16); }
DI u32 pk2(float a, float b) {
  f32x2 v = {a, b};
  return __builtin_bit_cast(u32, __builtin_convertvector(v, bf2));
}
DI float lo16(u32 v) { return __uint_as_float(v << 16); }
DI float hi16(u32 v) { return __uint_as_float(v & 0xffff0000u); }
DI float gelu(float x) {
  const float k0 = -2.302208198f, k1 = -0.1029432397f;
  float e = __builtin_amdgcn_exp2f(x * __builtin_fmaf(k1, x * x, k0));
  return x * __builtin_amdgcn_rcpf(1.0f + e);
}
struct cf { float re, im; };
DI cf cmul(cf a, cf b) { return {a.re * b.re - a.im * b.im, a.re * b.im + a.im * b.re}; }
DI cf cfma(cf a, cf b, cf c) {
  return {__builtin_fmaf(a.re, b.re, __builtin_fmaf(-a.im, b.im, c.re)), __builtin_fmaf(a.re, b.im, __builtin_fmaf(a.im, b.re, c.im))};
}
DI void sincos_cw(float x, float& s, float& c) {
  float k = __builtin_rintf(x * 0.6366197724f);
  float r = __builtin_fmaf(k, -1.57079637050628662109375f, x);
  r = __builtin_fmaf(k, 4.37113900018624283e-8f, r);
  float r2 = r * r;
  float ps = __builtin_fmaf(r2, __builtin_fmaf(r2, __builtin_fmaf(r2, 2.7557314297e-06f, -1.9841270114e-04f), 8.3333337680e-03f), -1.6666667163e-01f);
  float sr = __builtin_fmaf(r * r2, ps, r);
  float pc = __builtin_fmaf(r2, __builtin_fmaf(r2, __builtin_fmaf(r2, -2.7557314297e-07f, 2.4801587642e-05f), -1.3888889225e-03f), 4.1666667908e-02f);
  float cr = __builtin_fmaf(r2 * r2, pc, __builtin_fmaf(r2, -0.5f, 1.0f));
  int q = (int)k;
  float ss = (q & 1) ? cr : sr, cc = (q & 1) ? sr : cr;
  s = (q & 2) ? -ss : ss;
  c = ((q + 1) & 2) ? -cc : cc;
}
DI void pole(const u16* lre, const u16* lim, const u16* ldt, int p, cf& lam, cf& coef) {
  float lr = bf2f(lre[p]), li = bf2f(lim[p]);
  float dt = lo16(pk2(expf(bf2f(ldt[p])), 0.f));
  float e = expf(dt * lr), s, c;
  sincos_cw(dt * li, s, c);
  lam = {e * c, e * s};
  float inv = 1.0f / (lr * lr + li * li), nr = lam.re - 1.0f;
  coef = {(nr * lr + lam.im * li) * inv, (lam.im * lr - nr * li) * inv};
}
template <int CTRL>
DI float dpp(float v) {
  return __int_as_float(__builtin_amdgcn_update_dpp(0, __float_as_int(v), CTRL, 0xf, 0xf, true));
}
DI float reduce8(const float* v, int m) {
  float a[4], b[2];
  const bool h3 = m & 8, h2 = m & 4, h1 = m & 2;
#pragma unroll
  for (int k = 0; k < 4; k++) a[k] = (h3 ? v[k + 4] : v[k]) + dpp<0x140>(h3 ? v[k] : v[k + 4]);
#pragma unroll
  for (int k = 0; k < 2; k++) b[k] = (h2 ? a[k + 2] : a[k]) + dpp<0x141>(h2 ? a[k] : a[k + 2]);
  float c = (h1 ? b[1] : b[0]) + dpp<0x4E>(h1 ? b[0] : b[1]);
  return c + dpp<0xB1>(c);
}
DI bool flags_ok(u32x4 v, u32 lo, u32 hi, bool need) { return __all(!need || (v[0] == lo && v[1] == hi && v[2] == lo && v[3] == hi)); }
DI void spin_flags(u32 voff, i32x4 rs, u32 lo, u32 hi, bool need) {
  const u64 t0 = __builtin_amdgcn_s_memrealtime();
  for (;;) {
    u32x4 v = ld16_issue(voff, rs);
    WAITV1(0, v);
    if (flags_ok(v, lo, hi, need) || __builtin_amdgcn_s_memrealtime() - t0 > SPIN_TICKS) break;
    __builtin_amdgcn_s_sleep(2);
  }
}
DI void put_flag(char* ws, u32 off, u64 tag, int lane) {
  WAITV(0);
  if (lane == 0) __hip_atomic_store((GAS u64*)(ws + off), tag, __ATOMIC_RELAXED, __HIP_MEMORY_SCOPE_AGENT);
}
#define LSTAMP(k) do { if ((k) == AMPL) ampv = __builtin_amdgcn_s_memrealtime() - tstart; __builtin_amdgcn_s_waitcnt(0xC07F); } while (0)
#define STAMP(k)

__global__ __launch_bounds__(512, 2) void fused_kernel(const u16* x, const u16* lre, const u16* lim, const u16* bre, const u16* bim,
                                                       const u16* cre, const u16* cim, const u16* ldt, const u16* dsk, const u16* win,
                                                       const u16* wsp, const u16* bsp, const u16* wout, const u16* gw, float* out, char* ws) {
  extern __shared__ __attribute__((aligned(16))) char smem[];
  const int tid = threadIdx.x, wave = __builtin_amdgcn_readfirstlane(tid >> 6), lane = tid & 63;
  const int xcd = blockIdx.x & 7, idx = blockIdx.x >> 3;
  const int i2 = (xcd & 3) * 8 + (idx & 7), j2 = (xcd >> 2) * 4 + (idx >> 3);
  const u32 sb = (u32)(size_t)(LAS char*)smem;
  const int m = lane & 15, g = lane >> 4;
  const u64 tstart = __builtin_amdgcn_s_memrealtime();
  u64 ampv = 0;

  if (wave >= 4) {
    const int lw = wave - 4;
    const i32x4 rX = mkrsrc(x, 2048 * 512 * 2), rW = mkrsrc(win, 512 * 1376 * 2), rWS = mkrsrc(ws, WS_END);
    u32 vX[4], vW[6];
#pragma unroll
    for (int j = 0; j < 4; j++) vX[j] = (64 * i2 + 32 * (lw >> 1) + 8 * j + 4 * (lw & 1) + g) * 1024 + ((m ^ (4 * j + g)) << 4);
#pragma unroll
    for (int n = 0; n < 6; n++) {
      int c = n * 64 + lane, k = c / 12, cc = c - 12 * k;
      vW[n] = (32 * lw + k) * 2752 + 192 * j2 + ((cc ^ (((k >> 3) & 1) << 1)) << 4);
    }
    auto issueX = [&](u32 base, int s) {
#pragma unroll
      for (int j = 0; j < 4; j++) dma<0>(base + lw * 4096 + j * 1024, vX[j], rX, s * 256);
    };
    auto issueW = [&](u32 base, int s, u32 col) {
#pragma unroll
      for (int n = 0; n < 6; n++) dma<0>(base + 16384 + (lw * 6 + n) * 1024, vW[n], rW, s * (128 * 2752) + col);
    };
    u32x2 ep = ld8_issue(WS_EPOCH, rWS);
    if (lw == 0) dma<0>(sb + L_GW, lane * 16, mkrsrc(gw, 1024), 0);
    issueX(sb, 0);
    issueW(sb, 0, 2 * FH);
    issueX(sb + SLOT, 1);
    issueW(sb + SLOT, 1, 2 * FH);
    WAITV1(10, ep);
    const u64 tag = (((u64)__builtin_amdgcn_readfirstlane(ep[1]) << 32) | (u32)__builtin_amdgcn_readfirstlane(ep[0])) * 6364136223846793005ull + 1442695040888963407ull;
    const u32 tlo = (u32)tag, thi = (u32)(tag >> 32);

    const i32x4 rB = mkrsrc(lw >> 1 ? bim : bre, 512 * 512 * 2);
    u32 vB[8];
#pragma unroll
    for (int j = 0; j < 8; j++) {
      int ch = 32 * (lw & 1) + 4 * j + g;
      vB[j] = (64 * j2 + ch) * 1024 + ((m ^ (ch & 15)) << 4);
    }
    auto issueB = [&](u32 base, int s) {
#pragma unroll
      for (int j = 0; j < 8; j++) dma<0>(base + lw * 8192 + j * 1024, vB[j], rB, s * 256);
    };

    f32x4 gacc = {0.f, 0.f, 0.f, 0.f};
#pragma unroll
    for (int s = 0; s < 4; s++) {
      LBAR();
      LSTAMP(s);
      const u32 nb = sb + ((s + 2) % 3) * SLOT;
      if (s < 2) { issueX(nb, s + 2); issueW(nb, s + 2, 2 * FH); } else issueW(nb, s - 2, 0);
#pragma unroll
      for (int ks = 0; ks < 4; ks++)
        gacc = MFMA(lds16(sb + (s % 3) * SLOT + (16 * lw + m) * 256 + (((4 * ks + g) ^ m) << 4)), lds16(sb + L_GW + s * 256 + ks * 64 + g * 16), gacc);
      if (s < 2) WAITV(10); else WAITV(6);
    }
    {
      f32x4 gt;
#pragma unroll
      for (int r = 0; r < 4; r++) gt[r] = 1.0f / (1.0f + expf(-gacc[r]));
      if (m == 0) *(LAS f32x4*)(size_t)(sb + L_GATE + (16 * lw + 4 * g) * 4) = gt;
    }
    LBAR(); LSTAMP(4); issueW(sb + 0 * SLOT, 2, 0); WAITV(6);
    LBAR(); LSTAMP(5); issueW(sb + 1 * SLOT, 3, 0); WAITV(6);
    LBAR(); LSTAMP(6); issueB(sb + 2 * SLOT, 0); WAITV(8);
    LBAR(); LSTAMP(7);
    {
      const u32 vs = WS_STAT + ((i2 * 16 + 4 * g) * 64 + 16 * lw + m) * 16;
      u32x4 p0 = ld16_issue(vs, rWS), p1 = ld16_issue(vs + 1024, rWS), p2 = ld16_issue(vs + 2048, rWS), p3 = ld16_issue(vs + 3072, rWS);
      issueB(sb + 0 * SLOT, 1);
      WAITV4(8, p0, p1, p2, p3);
      const u64 t0 = __builtin_amdgcn_s_memrealtime();
      while (!__all(p0[1] == tlo && p0[3] == thi && p1[1] == tlo && p1[3] == thi && p2[1] == tlo && p2[3] == thi && p3[1] == tlo && p3[3] == thi)) {
        if (__builtin_amdgcn_s_memrealtime() - t0 > SPIN_TICKS) break;
        __builtin_amdgcn_s_sleep(1);
        p0 = ld16_issue(vs, rWS); p1 = ld16_issue(vs + 1024, rWS); p2 = ld16_issue(vs + 2048, rWS); p3 = ld16_issue(vs + 3072, rWS);
        WAITV4(0, p0, p1, p2, p3);
      }
      float s1 = (__uint_as_float(p0[0]) + __uint_as_float(p1[0])) + (__uint_as_float(p2[0]) + __uint_as_float(p3[0]));
      float s2 = (__uint_as_float(p0[2]) + __uint_as_float(p1[2])) + (__uint_as_float(p2[2]) + __uint_as_float(p3[2]));
      s1 += __shfl_xor(s1, 16); s2 += __shfl_xor(s2, 16);
      s1 += __shfl_xor(s1, 32); s2 += __shfl_xor(s2, 32);
      const float mean = s1 * (1.0f / FH), var = fmaxf(s2 * (1.0f / FH) - mean * mean, 0.f);
      const float rstd = 1.0f / sqrtf(var + 1e-5f);
      if (g == 0) *(LAS f32x2*)(size_t)(sb + L_STAT + (16 * lw + m) * 8) = f32x2{rstd, -mean * rstd};
    }
    const i32x4 rA = mkrsrc(wsp, 2048 * 2048 * 2);
    u32 vA[4];
#pragma unroll
    for (int j = 0; j < 4; j++) vA[j] = (64 * i2 + 32 * (lw >> 1) + 8 * j + 4 * (lw & 1) + g) * 4096 + ((m ^ (4 * j + g)) << 4);
    const u32 vZ = WS_Z + (lw * 48 + 6 * j2) * 1024 + lane * 16;
    auto issueA = [&](u32 base, int s) {
#pragma unroll
      for (int j = 0; j < 4; j++) dma<0>(base + lw * 4096 + j * 1024, vA[j], rA, s * 256);
    };
    auto issueZ = [&](u32 base, int s) {
#pragma unroll
      for (int n = 0; n < 6; n++) dma<1>(base + 16384 + (lw * 6 + n) * 1024, vZ, rWS, s * (4 * 48 * 1024) + n * 1024);
    };
    LBAR(); LSTAMP(8); issueB(sb + 1 * SLOT, 2); WAITV(8);
    LBAR(); LSTAMP(9); issueB(sb + 2 * SLOT, 3); WAITV(8);
    LBAR(); LSTAMP(10); issueA(sb + 0 * SLOT, 0); WAITV(4);
    LBAR(); LSTAMP(11);
    {
      const u32 vf = WS_F1 + j2 * 1024 + lane * 16;
      u32x4 f = ld16_issue(vf, rWS);
      issueA(sb + 1 * SLOT, 1);
      WAITV1(4, f);
      if (!flags_ok(f, tlo, thi, true)) spin_flags(vf, rWS, tlo, thi, true);
      LSTAMP(48);
      issueZ(sb + 0 * SLOT, 0);
      issueZ(sb + 1 * SLOT, 1);
      if (blockIdx.x == 0 && lw == 0 && lane == 0) __hip_atomic_store((GAS u64*)(ws + WS_EPOCH), tag, __ATOMIC_RELAXED, __HIP_MEMORY_SCOPE_AGENT);
      if (blockIdx.x == 0 && lw == 0) WAITV(7); else WAITV(6);
    }
    u32 nb = sb + 2 * SLOT;
    auto rot = [&]() { nb += SLOT; if (nb == sb + 3 * SLOT) nb = sb; };
    for (int s = 0; s < 3; s++) {
      LBAR(); LSTAMP(12 + s); issueZ(nb, s + 2); issueA(nb, s + 2); rot(); WAITV(10);
    }
    {
      LBAR(); LSTAMP(15);
      const u32 vf = WS_FW + j2 * 1024 + lane * 16;
      const bool need = (lane >> 1) <= i2;
      u32x4 f = ld16_issue(vf, rWS);
      issueZ(nb, 5); issueA(nb, 5); rot();
      WAITV1(10, f);
      if (!flags_ok(f, tlo, thi, need)) spin_flags(vf, rWS, tlo, thi, need);
      LBAR(); LSTAMP(16);
      u32x2 w[17];
      const u32 vs = WS_SEG + j2 * (64 * 512) + (16 * lw + m) * 8;
#pragma unroll
      for (int i = 0; i < 16; i++) {
        int sg = 2 * i2 - 64 + 16 * g + i;
        w[i] = ld8_issue(sg >= 0 ? vs + sg * 512 : 0x80000000u, rWS);
      }
      w[16] = ld8_issue(vs + 2 * i2 * 512, rWS);
      issueZ(nb, 6); issueA(nb, 6); rot();
      asm volatile("s_waitcnt vmcnt(10)"
                   : "+v"(w[0]), "+v"(w[1]), "+v"(w[2]), "+v"(w[3]), "+v"(w[4]), "+v"(w[5]), "+v"(w[6]), "+v"(w[7]), "+v"(w[8]), "+v"(w[9]),
                     "+v"(w[10]), "+v"(w[11]), "+v"(w[12]), "+v"(w[13]), "+v"(w[14]), "+v"(w[15]), "+v"(w[16])::"memory");
      LBAR(); LSTAMP(17);
      issueZ(nb, 7); issueA(nb, 7); rot();
      const f32x4 pw = *(const LAS f32x4*)(size_t)(sb + L_POW + 2048 + (16 * lw + m) * 16);
      const cf l32 = {pw[0], pw[1]}, l512 = {pw[2], pw[3]};
      cf h = {__uint_as_float(w[0][0]), __uint_as_float(w[0][1])};
#pragma unroll
      for (int i = 1; i < 16; i++) h = cfma(l32, h, cf{__uint_as_float(w[i][0]), __uint_as_float(w[i][1])});
      cf c0 = {__shfl(h.re, m), __shfl(h.im, m)};
#pragma unroll
      for (int k = 1; k < 4; k++) c0 = cfma(l512, c0, cf{__shfl(h.re, m + 16 * k), __shfl(h.im, m + 16 * k)});
      cf c1 = cfma(l32, c0, cf{__uint_as_float(w[16][0]), __uint_as_float(w[16][1])});
      if (g < 2) *(LAS f32x2*)(size_t)(sb + L_CARRY + g * 512 + (16 * lw + m) * 8) = g ? f32x2{c1.re, c1.im} : f32x2{c0.re, c0.im};
      WAITV(10);
    }
    for (int s = 6; s < 14; s++) {
      LBAR(); LSTAMP(12 + s); issueZ(nb, s + 2); issueA(nb, s + 2); rot(); WAITV(10);
    }
    const i32x4 rC0 = mkrsrc(cre, 512 * 512 * 2), rC1 = mkrsrc(cim, 512 * 512 * 2);
    u32 vC[4];
#pragma unroll
    for (int j = 0; j < 4; j++) vC[j] = (64 * j2 + 16 * lw + 4 * j + g) * 1024 + ((m ^ (4 * j + g)) << 4);
    const u32 vH = WS_H + ((2 * i2 + (lw >> 1)) * 32 + 4 * (lw & 1)) * 1024 + lane * 16;
    auto issueC = [&](u32 base, int s) {
#pragma unroll
      for (int j = 0; j < 4; j++) {
        if (s < 4) dma<0>(base + 16384 + lw * 4096 + j * 1024, vC[j], rC0, s * 256);
        else dma<0>(base + 16384 + lw * 4096 + j * 1024, vC[j], rC1, (s - 4) * 256);
      }
    };
    auto issueH = [&](u32 base, int s) {
#pragma unroll
      for (int j = 0; j < 4; j++) dma<1>(base + lw * 4096 + j * 1024, vH, rWS, (s >> 2) * (64 * 32 * 1024) + (s & 3) * 8192 + j * 1024);
    };
    {
      LBAR(); LSTAMP(26);
      const u32 vf = WS_F2 + i2 * 256 + m * 16;
      u32x4 f = ld16_issue(vf, rWS);
      issueC(nb, 0);
      WAITV1(4, f);
      if (!flags_ok(f, tlo, thi, true)) spin_flags(vf, rWS, tlo, thi, true);
      issueH(nb, 0); rot();
      WAITV(8);
    }
    for (int s = 1; s < 4; s++) {
      LBAR(); LSTAMP(26 + s); issueH(nb, s); issueC(nb, s); rot(); WAITV(8);
    }
    for (int s = 4; s < 8; s++) {
      LBAR(); LSTAMP(26 + s); issueH(nb, s); issueC(nb, s); rot(); WAITV(8);
    }
    const i32x4 rO = mkrsrc(wout, FH * 512 * 2);
    u32 vO[4];
#pragma unroll
    for (int j = 0; j < 4; j++) {
      int row = 32 * lw + 8 * j + (lane >> 3);
      vO[j] = row * 1024 + j2 * 128 + (((lane & 7) ^ (((row >> 1) & 3) << 1)) << 4);
    }
    const u32 vV = WS_V + ((2 * i2 + (lw >> 1)) * 48 + 4 * (lw & 1)) * 1024 + lane * 16;
    auto issueO = [&](u32 base, int s) {
#pragma unroll
      for (int j = 0; j < 4; j++) dma<0>(base + 16384 + lw * 4096 + j * 1024, vO[j], rO, s * (128 * 1024));
    };
    auto issueV = [&](u32 base, int s) {
#pragma unroll
      for (int j = 0; j < 4; j++) dma<1>(base + lw * 4096 + j * 1024, vV, rWS, s * 8192 + j * 1024);
    };
    {
      LBAR(); LSTAMP(34);
      const u32 vf = WS_F3 + i2 * 256 + m * 16;
      u32x4 f = ld16_issue(vf, rWS);
      issueO(nb, 0);
      WAITV1(4, f);
      if (!flags_ok(f, tlo, thi, true)) spin_flags(vf, rWS, tlo, thi, true);
      LSTAMP(49);
      issueV(nb, 0); rot();
      WAITV(8);
    }
    for (int s = 1; s < 5; s++) {
      LBAR(); LSTAMP(34 + s); issueV(nb, s); issueO(nb, s); rot(); WAITV(8);
    }
    LBAR(); LSTAMP(39);
    if ((lw & 1) == 0) issueV(nb, 5);
    if (lw < 2) issueO(nb, 5);
    if (lw == 0) WAITV(8); else if (lw == 3) WAITV(0); else WAITV(4);
    LBAR(); LSTAMP(40);
    WAITV(0);
    LBAR(); LSTAMP(41);
    { u64 t0 = __builtin_amdgcn_s_memrealtime(); while (__builtin_amdgcn_s_memrealtime() - t0 < ampv * 100) __builtin_amdgcn_s_sleep(8); }
  } else {
    const int wr = wave >> 1, wc = wave & 1, qq = m >> 2, p = lane & 3;
    const __amdgpu_buffer_rsrc_t rS = __builtin_amdgcn_make_buffer_rsrc(ws, 0, WS_END, 0x00020000);
    const u64 ep = __hip_atomic_load((GAS u64*)(ws + WS_EPOCH), __ATOMIC_RELAXED, __HIP_MEMORY_SCOPE_AGENT);
    const int trow = 64 * i2 + 32 * wr + 8 * (m >> 2) + (m & 3);
    const float bias0 = bf2f(bsp[trow]), bias1 = bf2f(bsp[trow + 4]);
    if (wave == 0) {
      cf lam, coef;
      pole(lre, lim, ldt, 64 * j2 + lane, lam, coef);
      cf t = cmul(lam, lam);
      t = cmul(t, t);
      const cf l8 = cmul(t, t), l16 = cmul(l8, l8), l32 = cmul(l16, l16);
      t = cmul(l32, l32);
      t = cmul(t, t);
      t = cmul(t, t);
      t = cmul(t, t);
      *(LAS f32x4*)(size_t)(sb + L_POW + lane * 16) = f32x4{lam.re, lam.im, 2.f * coef.re, 2.f * coef.im};
      *(LAS f32x4*)(size_t)(sb + L_POW + 1024 + lane * 16) = f32x4{l8.re, l8.im, l16.re, l16.im};
      *(LAS f32x4*)(size_t)(sb + L_POW + 2048 + lane * 16) = f32x4{l32.re, l32.im, t.re, t.im};
    }
    const u64 tag = (((u64)__builtin_amdgcn_readfirstlane((u32)(ep >> 32)) << 32) | (u32)__builtin_amdgcn_readfirstlane((u32)ep)) * 6364136223846793005ull + 1442695040888963407ull;
    const u32 tlo = (u32)tag, thi = (u32)(tag >> 32);
    const int tile0 = 6 * j2 + 3 * wc;
    const int rg = 2 * i2 + wr;

    u32 aR[4], aW[3];
#pragma unroll
    for (int ks = 0; ks < 4; ks++) aR[ks] = sb + m * 256 + (((4 * ks + g) ^ m) << 4);
#pragma unroll
    for (int n = 0; n < 3; n++) aW[n] = sb + 16384 + (8 * g + qq) * 192 + (((3 * wc + n) ^ (g & 1)) << 5) + p * 8;

    bf16x8 xf[2][16];
    bf16x8 wf[2][3];
    auto rdW = [&](bf16x8* w, int kk, int st) {
      const u32 o = (st % 3) * SLOT + (kk & 3) * 6144;
#pragma unroll
      for (int n = 0; n < 3; n++) w[n] = cat(ldstr(aW[n] + o), ldstr(aW[n] + o + 768));
    };
    auto rdX = [&](int kk) {
#pragma unroll
      for (int u = 0; u < 2; u++) xf[u][kk] = lds16(aR[kk & 3] + ((kk >> 2) % 3) * SLOT + (32 * wr + 16 * u) * 256);
    };

    f32x4 z2[2][3] = {};
    __builtin_amdgcn_s_waitcnt(0xC07F);
    LBAR();
    rdX(0); rdW(wf[0], 0, 0);
#pragma unroll
    for (int kk = 0; kk < 16; kk++) {
      if (kk < 15) {
        if ((kk & 3) == 3) LBAR();
        rdX(kk + 1); rdW(wf[(kk + 1) & 1], kk + 1, (kk + 1) >> 2);
      }
      SB();
#pragma unroll
      for (int u = 0; u < 2; u++)
#pragma unroll
        for (int n = 0; n < 3; n++) z2[u][n] = MFMA(xf[u][kk], wf[kk & 1][n], z2[u][n]);
      SB();
    }
    STAMP(0);
    u32 z2p[3][4];
    {
      float s1[8], s2[8];
#pragma unroll
      for (int j = 0; j < 8; j++) { s1[j] = 0.f; s2[j] = 0.f; }
#pragma unroll
      for (int n = 0; n < 3; n++) {
        const bool ok = tile0 + n < FH / 16;
#pragma unroll
        for (int j = 0; j < 8; j++) {
          float v = ok ? gelu(z2[j >> 2][n][j & 3]) : 0.f;
          z2[j >> 2][n][j & 3] = v;
          s1[j] += v;
          s2[j] = __builtin_fmaf(v, v, s2[j]);
        }
#pragma unroll
        for (int k = 0; k < 4; k++) z2p[n][k] = pk2(z2[k >> 1][n][(2 * k) & 3], z2[k >> 1][n][(2 * k + 1) & 3]);
      }
      const float r1 = reduce8(s1, m), r2 = reduce8(s2, m);
      if (!(m & 1))
        __builtin_amdgcn_raw_buffer_store_b128(u32x4{__float_as_uint(r1), tlo, __float_as_uint(r2), thi}, rS,
                                               WS_STAT + ((i2 * 16 + 2 * j2 + wc) * 64 + 32 * wr + 8 * g + (m >> 1)) * 16, 0, 16);
    }
    STAMP(1);
    u32 z1p[2][3][2];
    {
      f32x4 z1[2][3] = {};
      LBAR();
      rdW(wf[0], 0, 4);
#pragma unroll
      for (int kk = 0; kk < 16; kk++) {
        if (kk < 15) {
          if ((kk & 3) == 3) LBAR();
          rdW(wf[(kk + 1) & 1], kk + 1, 4 + ((kk + 1) >> 2));
        }
        SB();
#pragma unroll
        for (int u = 0; u < 2; u++)
#pragma unroll
          for (int n = 0; n < 3; n++) z1[u][n] = MFMA(wf[kk & 1][n], xf[u][kk], z1[u][n]);
        SB();
      }
      STAMP(2);
#pragma unroll
      for (int u = 0; u < 2; u++)
#pragma unroll
        for (int n = 0; n < 3; n++) {
          z1p[u][n][0] = pk2(gelu(z1[u][n][0]), gelu(z1[u][n][1]));
          z1p[u][n][1] = pk2(gelu(z1[u][n][2]), gelu(z1[u][n][3]));
        }
    }
    STAMP(3);
    LBAR();
    STAMP(4);
    {
      const u32 as = sb + L_STAT + (32 * wr + 8 * g) * 8;
      f32x4 st[4];
#pragma unroll
      for (int k = 0; k < 4; k++) st[k] = *(const LAS f32x4*)(size_t)(as + k * 16);
#pragma unroll
      for (int n = 0; n < 3; n++) {
        const bool ok = tile0 + n < FH / 16;
        u32x4 o;
#pragma unroll
        for (int k = 0; k < 4; k++) {
          float a = __builtin_fmaf(lo16(z2p[n][k]), st[k][0], st[k][1]);
          float b = __builtin_fmaf(hi16(z2p[n][k]), st[k][2], st[k][3]);
          o[k] = ok ? pk2(a, b) : 0u;
        }
        __builtin_amdgcn_raw_buffer_store_b128(o, rS, WS_Z + (rg * 48 + tile0 + n) * 1024 + lane * 16, 0, 16);
      }
    }
    STAMP(5);
    f32x4 ur[2][2] = {}, ui[2][2] = {};
    {
      bf16x8 bf[2][4];
      auto rdB = [&](bf16x8* b, int kk) {
        const u32 o = ((8 + (kk >> 2)) % 3) * SLOT + 32 * wc * 256;
#pragma unroll
        for (int c = 0; c < 4; c++) b[c] = lds16(aR[kk & 3] + o + (c >> 1) * 16384 + (c & 1) * 4096);
      };
      rdB(bf[0], 0);
#pragma unroll
      for (int kk = 0; kk < 16; kk++) {
        if (kk < 15) {
          if ((kk & 3) == 3) LBAR();
          rdB(bf[(kk + 1) & 1], kk + 1);
        }
        SB();
#pragma unroll
        for (int u = 0; u < 2; u++)
#pragma unroll
          for (int a = 0; a < 2; a++) {
            ur[u][a] = MFMA(xf[u][kk], bf[kk & 1][a], ur[u][a]);
            ui[u][a] = MFMA(xf[u][kk], bf[kk & 1][2 + a], ui[u][a]);
          }
        SB();
        if (kk == 2) put_flag(ws, WS_F1 + ((j2 * 32 + i2) * 4 + wave) * 8, tag, lane);
      }
    }
    STAMP(6);
    cf pre[2];
#pragma unroll
    for (int a = 0; a < 2; a++) {
      const u32 ap = sb + L_POW + (32 * wc + 16 * a + m) * 16;
      const f32x4 t0 = *(const LAS f32x4*)(size_t)ap, t1 = *(const LAS f32x4*)(size_t)(ap + 1024);
      const cf lam = {t0[0], t0[1]}, l8 = {t1[0], t1[1]}, l16 = {t1[2], t1[3]};
      cf e = {ur[0][a][0], ui[0][a][0]};
#pragma unroll
      for (int j = 1; j < 8; j++) e = cfma(lam, e, cf{ur[j >> 2][a][j & 3], ui[j >> 2][a][j & 3]});
      cf o = {__shfl_up(e.re, 16), __shfl_up(e.im, 16)};
      if (g >= 1) e = cfma(l8, o, e);
      o = {__shfl_up(e.re, 32), __shfl_up(e.im, 32)};
      if (g >= 2) e = cfma(l16, o, e);
      o = {__shfl_up(e.re, 16), __shfl_up(e.im, 16)};
      pre[a] = g ? o : cf{0.f, 0.f};
      if (g == 3)
        __builtin_amdgcn_raw_buffer_store_b64(u32x2{__float_as_uint(e.re), __float_as_uint(e.im)}, rS,
                                              WS_SEG + (j2 * 64 + rg) * 512 + (32 * wc + 16 * a + m) * 8, 0, 16);
    }
    STAMP(7);
    f32x4 ga[2][3] = {};
    {
      const u32 aZ = sb + 16384 + 3 * wc * 1024 + lane * 16;
      bf16x8 ca[3], cb[2], na[3], nbf[2];
      auto rd = [&](bf16x8* a, bf16x8* b, u32 so, int ks) {
        b[0] = lds16(aR[ks] + so + 32 * wr * 256);
#pragma unroll
        for (int n = 0; n < 3; n++) a[n] = lds16(aZ + so + (ks * 6 + n) * 1024);
        b[1] = lds16(aR[ks] + so + (32 * wr + 16) * 256);
      };
      auto mm = [&](bf16x8* a, bf16x8* b) {
#pragma unroll
        for (int u = 0; u < 2; u++)
#pragma unroll
          for (int n = 0; n < 3; n++) ga[u][n] = MFMA(a[n], b[u], ga[u][n]);
      };
      u32 so = 0;
      LBAR();
      STAMP(8);
      rd(ca, cb, so, 0);
      for (int s = 0; s < 16; s++) {
        rd(na, nbf, so, 1); SB(); mm(ca, cb); SB();
        rd(ca, cb, so, 2); SB(); mm(na, nbf); SB();
        rd(na, nbf, so, 3); SB(); mm(ca, cb); SB();
        if (s == 0) put_flag(ws, WS_FW + ((j2 * 32 + i2) * 4 + wave) * 8, tag, lane);
        if (s == 9) put_flag(ws, WS_F2 + ((i2 * 8 + j2) * 4 + wave) * 8, tag, lane);
        if (s == 8) {
#pragma unroll
          for (int a = 0; a < 2; a++) {
            const f32x2 cw = *(const LAS f32x2*)(size_t)(sb + L_CARRY + wr * 512 + (32 * wc + 16 * a + m) * 8);
            const u32 ap = sb + L_POW + (32 * wc + 16 * a + m) * 16;
            const f32x4 t0 = *(const LAS f32x4*)(size_t)ap, t1 = *(const LAS f32x4*)(size_t)(ap + 1024);
            const cf lam = {t0[0], t0[1]}, c2 = {t0[2], t0[3]};
            cf lq = (g & 1) ? cf{t1[0], t1[1]} : cf{1.f, 0.f};
            if (g & 2) lq = cmul(lq, cf{t1[2], t1[3]});
            cf h = cfma(lq, cf{cw[0], cw[1]}, pre[a]);
            float hr[8], hi[8];
#pragma unroll
            for (int j = 0; j < 8; j++) {
              h = cfma(lam, h, cf{ur[j >> 2][a][j & 3], ui[j >> 2][a][j & 3]});
              hr[j] = c2.re * h.re - c2.im * h.im;
              hi[j] = -(c2.re * h.im + c2.im * h.re);
            }
            const bool sw = m & 8;
            const u32 r0 = pk2(hr[0], hr[1]), r1 = pk2(hr[2], hr[3]), r2 = pk2(hr[4], hr[5]), r3 = pk2(hr[6], hr[7]);
            const u32 i0 = pk2(hi[0], hi[1]), i1 = pk2(hi[2], hi[3]), i2_ = pk2(hi[4], hi[5]), i3 = pk2(hi[6], hi[7]);
            const u32 off = WS_H + (rg * 32 + 4 * j2 + 2 * wc + a) * 1024 + (m * 4 + g) * 16;
            __builtin_amdgcn_raw_buffer_store_b128(sw ? u32x4{r2, r3, r0, r1} : u32x4{r0, r1, r2, r3}, rS, off, 0, 16);
            __builtin_amdgcn_raw_buffer_store_b128(sw ? u32x4{i2_, i3, i0, i1} : u32x4{i0, i1, i2_, i3}, rS, off + 64 * 32 * 1024, 0, 16);
          }
        }
        so += SLOT;
        if (so == 3 * SLOT) so = 0;
        if (s < 15) { LBAR(); rd(ca, cb, so, 0); }
        SB(); mm(na, nbf); SB();
      }
    }
    STAMP(9);
#pragma unroll
    for (int n = 0; n < 3; n++) {
      if (tile0 + n < 44) {
        const bool ok = tile0 + n < FH / 16;
        u32 h[2][2];
#pragma unroll
        for (int u = 0; u < 2; u++) {
          const float b = u ? bias1 : bias0;
          h[u][0] = pk2(lo16(z1p[u][n][0]) * (ga[u][n][0] + b), hi16(z1p[u][n][0]) * (ga[u][n][1] + b));
          h[u][1] = pk2(lo16(z1p[u][n][1]) * (ga[u][n][2] + b), hi16(z1p[u][n][1]) * (ga[u][n][3] + b));
        }
        u32x4 o = (g & 1) ? u32x4{h[1][0], h[1][1], h[0][0], h[0][1]} : u32x4{h[0][0], h[0][1], h[1][0], h[1][1]};
        if (!ok) o = u32x4{0u, 0u, 0u, 0u};
        __builtin_amdgcn_raw_buffer_store_b128(o, rS, WS_V + (rg * 48 + tile0 + n) * 1024 + lane * 16, 0, 16);
      }
    }
    STAMP(10);
    f32x4 ya[2][2] = {}, yb[2][2] = {};
    const int dcol = 64 * j2 + 32 * wc + 4 * g;
    u32x2 xe[2][2], de[2];
    {
      u32 aH[2];
#pragma unroll
      for (int u = 0; u < 2; u++) aH[u] = sb + (wr * 8 + (g >> 1)) * 1024 + (8 * (g & 1) + qq) * 64 + p * 16 + ((u ^ (g & 1)) << 3);
      bf16x8 ca[2], cb[2], na[2], nbf[2];
      auto rd = [&](bf16x8* a, bf16x8* b, u32 so, int ks) {
#pragma unroll
        for (int u = 0; u < 2; u++) {
          b[u] = cat(ldstr(aH[u] + so + ks * 2048), ldstr(aH[u] + so + ks * 2048 + 256));
          a[u] = lds16(aR[ks] + so + 16384 + (32 * wc + 16 * u) * 256);
        }
      };
      auto mm = [&](bf16x8* a, bf16x8* b) {
#pragma unroll
        for (int u = 0; u < 2; u++)
#pragma unroll
          for (int n = 0; n < 2; n++) ya[u][n] = MFMA(a[n], b[u], ya[u][n]);
      };
      u32 so = SLOT;
      LBAR();
      rd(ca, cb, so, 0);
      for (int s = 0; s < 8; s++) {
        rd(na, nbf, so, 1); SB(); mm(ca, cb); SB();
        rd(ca, cb, so, 2); SB(); mm(na, nbf); SB();
        rd(na, nbf, so, 3); SB(); mm(ca, cb); SB();
        if (s == 0) put_flag(ws, WS_F3 + ((i2 * 8 + j2) * 4 + wave) * 8, tag, lane);
        so += SLOT;
        if (so == 3 * SLOT) so = 0;
        if (s < 7) { LBAR(); rd(ca, cb, so, 0); }
        SB(); mm(na, nbf); SB();
      }
    }
    STAMP(11);
#pragma unroll
    for (int n = 0; n < 2; n++) {
      de[n] = *(const u32x2*)(dsk + dcol + 16 * n);
#pragma unroll
      for (int u = 0; u < 2; u++) xe[u][n] = *(const u32x2*)(x + (trow + 4 * u) * 512 + dcol + 16 * n);
    }
    {
      u32 aV[2], aO[2];
#pragma unroll
      for (int u = 0; u < 2; u++) {
        aV[u] = sb + wr * 8192 + lane * 16 + ((u ^ (g & 1)) << 3);
        aO[u] = sb + 16384 + (4 * g + qq) * 128 + (((2 * wc + u) ^ (2 * (g & 1) + (qq >> 1))) << 5) + p * 8;
      }
      bf16x8 ca[2], cb[2], na[2], nbf[2];
      auto rd = [&](bf16x8* a, bf16x8* b, u32 so, int ks) {
#pragma unroll
        for (int u = 0; u < 2; u++) {
          b[u] = cat(lds8(aV[u] + so + ks * 2048), lds8(aV[u] + so + ks * 2048 + 1024));
          a[u] = cat(ldstr(aO[u] + so + ks * 4096), ldstr(aO[u] + so + ks * 4096 + 2048));
        }
      };
      auto mm = [&](bf16x8* a, bf16x8* b) {
#pragma unroll
        for (int u = 0; u < 2; u++)
#pragma unroll
          for (int n = 0; n < 2; n++) yb[u][n] = MFMA(a[n], b[u], yb[u][n]);
      };
      u32 so = 0;
      LBAR();
      rd(ca, cb, so, 0);
      for (int s = 0; s < 5; s++) {
        rd(na, nbf, so, 1); SB(); mm(ca, cb); SB();
        rd(ca, cb, so, 2); SB(); mm(na, nbf); SB();
        rd(na, nbf, so, 3); SB(); mm(ca, cb); SB();
        so += SLOT;
        if (so == 3 * SLOT) so = 0;
        LBAR(); rd(ca, cb, so, 0);
        SB(); mm(na, nbf); SB();
      }
      rd(na, nbf, so, 1); SB(); mm(ca, cb); SB();
      mm(na, nbf);
    }
    STAMP(12);
#pragma unroll
    for (int u = 0; u < 2; u++) {
      const float gt = *(const LAS float*)(size_t)(sb + L_GATE + (32 * wr + 16 * u + m) * 4);
#pragma unroll
      for (int n = 0; n < 2; n++) {
        f32x4 o;
        o[0] = __builtin_fmaf(lo16(xe[u][n][0]), lo16(de[n][0]), ya[u][n][0]);
        o[1] = __builtin_fmaf(hi16(xe[u][n][0]), hi16(de[n][0]), ya[u][n][1]);
        o[2] = __builtin_fmaf(lo16(xe[u][n][1]), lo16(de[n][1]), ya[u][n][2]);
        o[3] = __builtin_fmaf(hi16(xe[u][n][1]), hi16(de[n][1]), ya[u][n][3]);
#pragma unroll
        for (int r = 0; r < 4; r++) o[r] = gt * o[r] + (1.0f - gt) * yb[u][n][r];
        *(f32x4*)(out + (trow + 4 * u) * 512 + dcol + 16 * n) = o;
      }
    }
    STAMP(13);
  }
}
}

extern "C" void kernel_launch(void* const* d_in, const int* in_sizes, int n_in, void* d_out, int out_size, void* d_ws, size_t ws_size,
                              hipStream_t stream) {
  (void)hipFuncSetAttribute((const void*)fused_kernel, hipFuncAttributeMaxDynamicSharedMemorySize, LDS_BYTES);
  fused_kernel<<<256, 512, LDS_BYTES, stream>>>((const u16*)d_in[0], (const u16*)d_in[1], (const u16*)d_in[2], (const u16*)d_in[3],
                                                (const u16*)d_in[4], (const u16*)d_in[5], (const u16*)d_in[6], (const u16*)d_in[7],
                                                (const u16*)d_in[8], (const u16*)d_in[10], (const u16*)d_in[11], (const u16*)d_in[12],
                                                (const u16*)d_in[13], (const u16*)d_in[14], (float*)d_out, (char*)d_ws);
}
```

```cpp
#include <hip/hip_runtime.h>
#include <stdint.h>

namespace {
typedef unsigned long long u64;
typedef unsigned short u16;
typedef unsigned u32;
typedef short bf16x8 __attribute__((ext_vector_type(8)));
typedef short s16x4 __attribute__((ext_vector_type(4)));
typedef float f32x4 __attribute__((ext_vector_type(4)));
typedef float f32x2 __attribute__((ext_vector_type(2)));
typedef __bf16 bf2 __attribute__((ext_vector_type(2)));
typedef int i32x4 __attribute__((ext_vector_type(4)));
typedef u32 u32x4 __attribute__((ext_vector_type(4)));
typedef u32 u32x2 __attribute__((ext_vector_type(2)));
#define LAS __attribute__((address_space(3)))
#define GAS __attribute__((address_space(1)))
#define DI __device__ __forceinline__

constexpr int FH = 688;
constexpr u32 SLOT = 40960;
constexpr u32 L_GW = 3 * SLOT, L_STAT = L_GW + 1024, L_GATE = L_STAT + 512, L_CARRY = L_GATE + 256;
constexpr u32 L_POW = L_CARRY + 1024;
constexpr u32 LDS_BYTES = L_POW + 3072;

constexpr u32 WS_EPOCH = 0;
constexpr u32 WS_F1 = 4096, WS_FW = WS_F1 + 8192, WS_F2 = WS_FW + 8192, WS_F3 = WS_F2 + 8192;
constexpr u32 WS_STAT = 65536;
constexpr u32 WS_SEG = WS_STAT + 32 * 16 * 64 * 16;
constexpr u32 WS_Z = 1u << 20;
constexpr u32 WS_H = WS_Z + 64 * 48 * 1024;
constexpr u32 WS_V = WS_H + 2 * 64 * 32 * 1024;
constexpr u32 WS_END = WS_V + 64 * 48 * 1024;
constexpr u32 WS_STAMP = 16u << 20;
constexpr u64 SPIN_TICKS = 2000000;

DI i32x4 mkrsrc(const void* p, u32 bytes) {
  u64 a = (u64)p;
  i32x4 r;
  r[0] = __builtin_amdgcn_readfirstlane((u32)a);
  r[1] = __builtin_amdgcn_readfirstlane((u32)(a >> 32) & 0xffff);
  r[2] = bytes;
  r[3] = 0x00020000;
  return r;
}
template <int SC1>
DI void dma(u32 lds, u32 voff, i32x4 rs, u32 soff) {
  if constexpr (SC1)
    asm volatile("s_mov_b32 m0, %0\n\ts_nop 0\n\tbuffer_load_dwordx4 %1, %2, %3 offen sc1 lds" ::"s"(lds), "v"(voff), "s"(rs), "s"(soff) : "memory");
  else
    asm volatile("s_mov_b32 m0, %0\n\ts_nop 0\n\tbuffer_load_dwordx4 %1, %2, %3 offen lds" ::"s"(lds), "v"(voff), "s"(rs), "s"(soff) : "memory");
}
DI u32x4 ld16_issue(u32 voff, i32x4 rs) {
  u32x4 r;
  asm volatile("buffer_load_dwordx4 %0, %1, %2, 0 offen sc1" : "=v"(r) : "v"(voff), "s"(rs) : "memory");
  return r;
}
DI u32x2 ld8_issue(u32 voff, i32x4 rs) {
  u32x2 r;
  asm volatile("buffer_load_dwordx2 %0, %1, %2, 0 offen sc1" : "=v"(r) : "v"(voff), "s"(rs) : "memory");
  return r;
}
#define WAITV(n) asm volatile("s_waitcnt vmcnt(%0)" ::"n"(n) : "memory")
#define WAITV1(n, a) asm volatile("s_waitcnt vmcnt(%1)" : "+v"(a) : "n"(n) : "memory")
#define WAITV4(n, a, b, c, d) asm volatile("s_waitcnt vmcnt(%4)" : "+v"(a), "+v"(b), "+v"(c), "+v"(d) : "n"(n) : "memory")
#define LBAR() asm volatile("s_waitcnt lgkmcnt(0)\n\ts_barrier" ::: "memory")
#define SB() __builtin_amdgcn_sched_barrier(0)
#define MFMA(a, b, c) __builtin_amdgcn_mfma_f32_16x16x32_bf16((a), (b), (c), 0, 0, 0)

DI bf16x8 lds16(u32 a) { return *(const LAS bf16x8*)(size_t)a; }
DI s16x4 lds8(u32 a) { return *(const LAS s16x4*)(size_t)a; }
DI s16x4 ldstr(u32 a) { return __builtin_amdgcn_ds_read_tr16_b64_v4i16((LAS s16x4*)(size_t)a); }
DI bf16x8 cat(s16x4 lo, s16x4 hi) { return __builtin_shufflevector(lo, hi, 0, 1, 2, 3, 4, 5, 6, 7); }
DI float bf2f(u16 v) { return __uint_as_float((u32)v << 16); }
DI u32 pk2(float a, float b) {
  f32x2 v = {a, b};
  return __builtin_bit_cast(u32, __builtin_convertvector(v, bf2));
}
DI float lo16(u32 v) { return __uint_as_float(v << 16); }
DI float hi16(u32 v) { return __uint_as_float(v & 0xffff0000u); }
DI float gelu(float x) {
  const float k0 = -2.302208198f, k1 = -0.1029432397f;
  float e = __builtin_amdgcn_exp2f(x * __builtin_fmaf(k1, x * x, k0));
  return x * __builtin_amdgcn_rcpf(1.0f + e);
}
struct cf { float re, im; };
DI cf cmul(cf a, cf b) { return {a.re * b.re - a.im * b.im, a.re * b.im + a.im * b.re}; }
DI cf cfma(cf a, cf b, cf c) {
  return {__builtin_fmaf(a.re, b.re, __builtin_fmaf(-a.im, b.im, c.re)), __builtin_fmaf(a.re, b.im, __builtin_fmaf(a.im, b.re, c.im))};
}
DI void sincos_cw(float x, float& s, float& c) {
  float k = __builtin_rintf(x * 0.6366197724f);
  float r = __builtin_fmaf(k, -1.57079637050628662109375f, x);
  r = __builtin_fmaf(k, 4.37113900018624283e-8f, r);
  float r2 = r * r;
  float ps = __builtin_fmaf(r2, __builtin_fmaf(r2, __builtin_fmaf(r2, 2.7557314297e-06f, -1.9841270114e-04f), 8.3333337680e-03f), -1.6666667163e-01f);
  float sr = __builtin_fmaf(r * r2, ps, r);
  float pc = __builtin_fmaf(r2, __builtin_fmaf(r2, __builtin_fmaf(r2, -2.7557314297e-07f, 2.4801587642e-05f), -1.3888889225e-03f), 4.1666667908e-02f);
  float cr = __builtin_fmaf(r2 * r2, pc, __builtin_fmaf(r2, -0.5f, 1.0f));
  int q = (int)k;
  float ss = (q & 1) ? cr : sr, cc = (q & 1) ? sr : cr;
  s = (q & 2) ? -ss : ss;
  c = ((q + 1) & 2) ? -cc : cc;
}
DI void pole(const u16* lre, const u16* lim, const u16* ldt, int p, cf& lam, cf& coef) {
  float lr = bf2f(lre[p]), li = bf2f(lim[p]);
  float dt = lo16(pk2(expf(bf2f(ldt[p])), 0.f));
  float e = expf(dt * lr), s, c;
  sincos_cw(dt * li, s, c);
  lam = {e * c, e * s};
  float inv = 1.0f / (lr * lr + li * li), nr = lam.re - 1.0f;
  coef = {(nr * lr + lam.im * li) * inv, (lam.im * lr - nr * li) * inv};
}
template <int CTRL>
DI float dpp(float v) {
  return __int_as_float(__builtin_amdgcn_update_dpp(0, __float_as_int(v), CTRL, 0xf, 0xf, true));
}
DI float reduce8(const float* v, int m) {
  float a[4], b[2];
  const bool h3 = m & 8, h2 = m & 4, h1 = m & 2;
#pragma unroll
  for (int k = 0; k < 4; k++) a[k] = (h3 ? v[k + 4] : v[k]) + dpp<0x140>(h3 ? v[k] : v[k + 4]);
#pragma unroll
  for (int k = 0; k < 2; k++) b[k] = (h2 ? a[k + 2] : a[k]) + dpp<0x141>(h2 ? a[k] : a[k + 2]);
  float c = (h1 ? b[1] : b[0]) + dpp<0x4E>(h1 ? b[0] : b[1]);
  return c + dpp<0xB1>(c);
}
DI bool flags_ok(u32x4 v, u32 lo, u32 hi, bool need) { return __all(!need || (v[0] == lo && v[1] == hi && v[2] == lo && v[3] == hi)); }
DI void spin_flags(u32 voff, i32x4 rs, u32 lo, u32 hi, bool need) {
  const u64 t0 = __builtin_amdgcn_s_memrealtime();
  for (;;) {
    u32x4 v = ld16_issue(voff, rs);
    WAITV1(0, v);
    if (flags_ok(v, lo, hi, need) || __builtin_amdgcn_s_memrealtime() - t0 > SPIN_TICKS) break;
    __builtin_amdgcn_s_sleep(2);
  }
}
DI void put_flag(char* ws, u32 off, u64 tag, int lane) {
  WAITV(0);
  if (lane == 0) __hip_atomic_store((GAS u64*)(ws + off), tag, __ATOMIC_RELAXED, __HIP_MEMORY_SCOPE_AGENT);
}
#define LSTAMP(k) STAMP(k)
#define STAMP(k)

__global__ __launch_bounds__(512, 2) void fused_kernel(const u16* x, const u16* lre, const u16* lim, const u16* bre, const u16* bim,
                                                       const u16* cre, const u16* cim, const u16* ldt, const u16* dsk, const u16* win,
                                                       const u16* wsp, const u16* bsp, const u16* wout, const u16* gw, float* out, char* ws) {
  extern __shared__ __attribute__((aligned(16))) char smem[];
  const int tid = threadIdx.x, wave = __builtin_amdgcn_readfirstlane(tid >> 6), lane = tid & 63;
  const int xcd = blockIdx.x & 7, idx = blockIdx.x >> 3;
  const int i2 = (xcd & 3) * 8 + (idx & 7), j2 = (xcd >> 2) * 4 + (idx >> 3);
  const u32 sb = (u32)(size_t)(LAS char*)smem;
  const int m = lane & 15, g = lane >> 4;

  if (wave >= 4) {
    const int lw = wave - 4;
    const i32x4 rX = mkrsrc(x, 2048 * 512 * 2), rW = mkrsrc(win, 512 * 1376 * 2), rWS = mkrsrc(ws, WS_END);
    u32 vX[4], vW[6];
#pragma unroll
    for (int j = 0; j < 4; j++) vX[j] = (64 * i2 + 32 * (lw >> 1) + 8 * j + 4 * (lw & 1) + g) * 1024 + ((m ^ (4 * j + g)) << 4);
#pragma unroll
    for (int n = 0; n < 6; n++) {
      int c = n * 64 + lane, k = c / 12, cc = c - 12 * k;
      vW[n] = (32 * lw + k) * 2752 + 192 * j2 + ((cc ^ (((k >> 3) & 1) << 1)) << 4);
    }
    auto issueX = [&](u32 base, int s) {
#pragma unroll
      for (int j = 0; j < 4; j++) dma<0>(base + lw * 4096 + j * 1024, vX[j], rX, s * 256);
    };
    auto issueW = [&](u32 base, int s, u32 col) {
#pragma unroll
      for (int n = 0; n < 6; n++) dma<0>(base + 16384 + (lw * 6 + n) * 1024, vW[n], rW, s * (128 * 2752) + col);
    };
    u32x2 ep = ld8_issue(WS_EPOCH, rWS);
    if (lw == 0) dma<0>(sb + L_GW, lane * 16, mkrsrc(gw, 1024), 0);
    issueX(sb, 0);
    issueW(sb, 0, 2 * FH);
    issueX(sb + SLOT, 1);
    issueW(sb + SLOT, 1, 2 * FH);
    WAITV1(10, ep);
    const u64 tag = (((u64)__builtin_amdgcn_readfirstlane(ep[1]) << 32) | (u32)__builtin_amdgcn_readfirstlane(ep[0])) * 6364136223846793005ull + 1442695040888963407ull;
    const u32 tlo = (u32)tag, thi = (u32)(tag >> 32);

    const i32x4 rB = mkrsrc(lw >> 1 ? bim : bre, 512 * 512 * 2);
    u32 vB[8];
#pragma unroll
    for (int j = 0; j < 8; j++) {
      int ch = 32 * (lw & 1) + 4 * j + g;
      vB[j] = (64 * j2 + ch) * 1024 + ((m ^ (ch & 15)) << 4);
    }
    auto issueB = [&](u32 base, int s) {
#pragma unroll
      for (int j = 0; j < 8; j++) dma<0>(base + lw * 8192 + j * 1024, vB[j], rB, s * 256);
    };

    f32x4 gacc = {0.f, 0.f, 0.f, 0.f};
#pragma unroll
    for (int s = 0; s < 4; s++) {
      LBAR();
      LSTAMP(s);
      const u32 nb = sb + ((s + 2) % 3) * SLOT;
      if (s < 2) { issueX(nb, s + 2); issueW(nb, s + 2, 2 * FH); } else issueW(nb, s - 2, 0);
#pragma unroll
      for (int ks = 0; ks < 4; ks++)
        gacc = MFMA(lds16(sb + (s % 3) * SLOT + (16 * lw + m) * 256 + (((4 * ks + g) ^ m) << 4)), lds16(sb + L_GW + s * 256 + ks * 64 + g * 16), gacc);
      if (s < 2) WAITV(10); else WAITV(6);
    }
    {
      f32x4 gt;
#pragma unroll
      for (int r = 0; r < 4; r++) gt[r] = 1.0f / (1.0f + expf(-gacc[r]));
      if (m == 0) *(LAS f32x4*)(size_t)(sb + L_GATE + (16 * lw + 4 * g) * 4) = gt;
    }
    LBAR(); LSTAMP(4); issueW(sb + 0 * SLOT, 2, 0); WAITV(6);
    LBAR(); LSTAMP(5); issueW(sb + 1 * SLOT, 3, 0); WAITV(6);
    LBAR(); LSTAMP(6); issueB(sb + 2 * SLOT, 0); WAITV(8);
    LBAR(); LSTAMP(7);
    {
      const u32 vs = WS_STAT + ((i2 * 16 + 4 * g) * 64 + 16 * lw + m) * 16;
      u32x4 p0 = ld16_issue(vs, rWS), p1 = ld16_issue(vs + 1024, rWS), p2 = ld16_issue(vs + 2048, rWS), p3 = ld16_issue(vs + 3072, rWS);
      issueB(sb + 0 * SLOT, 1);
      WAITV4(8, p0, p1, p2, p3);
      const u64 t0 = __builtin_amdgcn_s_memrealtime();
      while (!__all(p0[1] == tlo && p0[3] == thi && p1[1] == tlo && p1[3] == thi && p2[1] == tlo && p2[3] == thi && p3[1] == tlo && p3[3] == thi)) {
        if (__builtin_amdgcn_s_memrealtime() - t0 > SPIN_TICKS) break;
        __builtin_amdgcn_s_sleep(1);
        p0 = ld16_issue(vs, rWS); p1 = ld16_issue(vs + 1024, rWS); p2 = ld16_issue(vs + 2048, rWS); p3 = ld16_issue(vs + 3072, rWS);
        WAITV4(0, p0, p1, p2, p3);
      }
      float s1 = (__uint_as_float(p0[0]) + __uint_as_float(p1[0])) + (__uint_as_float(p2[0]) + __uint_as_float(p3[0]));
      float s2 = (__uint_as_float(p0[2]) + __uint_as_float(p1[2])) + (__uint_as_float(p2[2]) + __uint_as_float(p3[2]));
      s1 += __shfl_xor(s1, 16); s2 += __shfl_xor(s2, 16);
      s1 += __shfl_xor(s1, 32); s2 += __shfl_xor(s2, 32);
      const float mean = s1 * (1.0f / FH), var = fmaxf(s2 * (1.0f / FH) - mean * mean, 0.f);
      const float rstd = 1.0f / sqrtf(var + 1e-5f);
      if (g == 0) *(LAS f32x2*)(size_t)(sb + L_STAT + (16 * lw + m) * 8) = f32x2{rstd, -mean * rstd};
    }
    const i32x4 rA = mkrsrc(wsp, 2048 * 2048 * 2);
    u32 vA[4];
#pragma unroll
    for (int j = 0; j < 4; j++) vA[j] = (64 * i2 + 32 * (lw >> 1) + 8 * j + 4 * (lw & 1) + g) * 4096 + ((m ^ (4 * j + g)) << 4);
    const u32 vZ = WS_Z + (lw * 48 + 6 * j2) * 1024 + lane * 16;
    auto issueA = [&](u32 base, int s) {
#pragma unroll
      for (int j = 0; j < 4; j++) dma<0>(base + lw * 4096 + j * 1024, vA[j], rA, s * 256);
    };
    auto issueZ = [&](u32 base, int s) {
#pragma unroll
      for (int n = 0; n < 6; n++) dma<1>(base + 16384 + (lw * 6 + n) * 1024, vZ, rWS, s * (4 * 48 * 1024) + n * 1024);
    };
    LBAR(); LSTAMP(8); issueB(sb + 1 * SLOT, 2); WAITV(8);
    LBAR(); LSTAMP(9); issueB(sb + 2 * SLOT, 3); WAITV(8);
    LBAR(); LSTAMP(10); issueA(sb + 0 * SLOT, 0); WAITV(4);
    LBAR(); LSTAMP(11);
    {
      const u32 vf = WS_F1 + j2 * 1024 + lane * 16;
      u32x4 f = ld16_issue(vf, rWS);
      issueA(sb + 1 * SLOT, 1);
      WAITV1(4, f);
      if (!flags_ok(f, tlo, thi, true)) spin_flags(vf, rWS, tlo, thi, true);
      LSTAMP(48);
      issueZ(sb + 0 * SLOT, 0);
      issueZ(sb + 1 * SLOT, 1);
      if (blockIdx.x == 0 && lw == 0 && lane == 0) __hip_atomic_store((GAS u64*)(ws + WS_EPOCH), tag, __ATOMIC_RELAXED, __HIP_MEMORY_SCOPE_AGENT);
      if (blockIdx.x == 0 && lw == 0) WAITV(7); else WAITV(6);
    }
    u32 nb = sb + 2 * SLOT;
    auto rot = [&]() { nb += SLOT; if (nb == sb + 3 * SLOT) nb = sb; };
    for (int s = 0; s < 3; s++) {
      LBAR(); LSTAMP(12 + s); issueZ(nb, s + 2); issueA(nb, s + 2); rot(); WAITV(10);
    }
    {
      LBAR(); LSTAMP(15);
      const u32 vf = WS_FW + j2 * 1024 + lane * 16;
      const bool need = (lane >> 1) <= i2;
      u32x4 f = ld16_issue(vf, rWS);
      issueZ(nb, 5); issueA(nb, 5); rot();
      WAITV1(10, f);
      if (!flags_ok(f, tlo, thi, need)) spin_flags(vf, rWS, tlo, thi, need);
      LBAR(); LSTAMP(16);
      u32x2 w[17];
      const u32 vs = WS_SEG + j2 * (64 * 512) + (16 * lw + m) * 8;
#pragma unroll
      for (int i = 0; i < 16; i++) {
        int sg = 2 * i2 - 64 + 16 * g + i;
        w[i] = ld8_issue(sg >= 0 ? vs + sg * 512 : 0x80000000u, rWS);
      }
      w[16] = ld8_issue(vs + 2 * i2 * 512, rWS);
      issueZ(nb, 6); issueA(nb, 6); rot();
      asm volatile("s_waitcnt vmcnt(10)"
                   : "+v"(w[0]), "+v"(w[1]), "+v"(w[2]), "+v"(w[3]), "+v"(w[4]), "+v"(w[5]), "+v"(w[6]), "+v"(w[7]), "+v"(w[8]), "+v"(w[9]),
                     "+v"(w[10]), "+v"(w[11]), "+v"(w[12]), "+v"(w[13]), "+v"(w[14]), "+v"(w[15]), "+v"(w[16])::"memory");
      LBAR(); LSTAMP(17);
      issueZ(nb, 7); issueA(nb, 7); rot();
      const f32x4 pw = *(const LAS f32x4*)(size_t)(sb + L_POW + 2048 + (16 * lw + m) * 16);
      const cf l32 = {pw[0], pw[1]}, l512 = {pw[2], pw[3]};
      cf h = {__uint_as_float(w[0][0]), __uint_as_float(w[0][1])};
#pragma unroll
      for (int i = 1; i < 16; i++) h = cfma(l32, h, cf{__uint_as_float(w[i][0]), __uint_as_float(w[i][1])});
      cf c0 = {__shfl(h.re, m), __shfl(h.im, m)};
#pragma unroll
      for (int k = 1; k < 4; k++) c0 = cfma(l512, c0, cf{__shfl(h.re, m + 16 * k), __shfl(h.im, m + 16 * k)});
      cf c1 = cfma(l32, c0, cf{__uint_as_float(w[16][0]), __uint_as_float(w[16][1])});
      if (g < 2) *(LAS f32x2*)(size_t)(sb + L_CARRY + g * 512 + (16 * lw + m) * 8) = g ? f32x2{c1.re, c1.im} : f32x2{c0.re, c0.im};
      WAITV(10);
    }
    for (int s = 6; s < 14; s++) {
      LBAR(); LSTAMP(12 + s); issueZ(nb, s + 2); issueA(nb, s + 2); rot(); WAITV(10);
    }
    const i32x4 rC0 = mkrsrc(cre, 512 * 512 * 2), rC1 = mkrsrc(cim, 512 * 512 * 2);
    u32 vC[4];
#pragma unroll
    for (int j = 0; j < 4; j++) vC[j] = (64 * j2 + 16 * lw + 4 * j + g) * 1024 + ((m ^ (4 * j + g)) << 4);
    const u32 vH = WS_H + ((2 * i2 + (lw >> 1)) * 32 + 4 * (lw & 1)) * 1024 + lane * 16;
    auto issueC = [&](u32 base, int s) {
#pragma unroll
      for (int j = 0; j < 4; j++) {
        if (s < 4) dma<0>(base + 16384 + lw * 4096 + j * 1024, vC[j], rC0, s * 256);
        else dma<0>(base + 16384 + lw * 4096 + j * 1024, vC[j], rC1, (s - 4) * 256);
      }
    };
    auto issueH = [&](u32 base, int s) {
#pragma unroll
      for (int j = 0; j < 4; j++) dma<1>(base + lw * 4096 + j * 1024, vH, rWS, (s >> 2) * (64 * 32 * 1024) + (s & 3) * 8192 + j * 1024);
    };
    {
      LBAR(); LSTAMP(26);
      const u32 vf = WS_F2 + i2 * 256 + m * 16;
      u32x4 f = ld16_issue(vf, rWS);
      issueC(nb, 0);
      WAITV1(4, f);
      if (!flags_ok(f, tlo, thi, true)) spin_flags(vf, rWS, tlo, thi, true);
      issueH(nb, 0); rot();
      WAITV(8);
    }
    for (int s = 1; s < 4; s++) {
      LBAR(); LSTAMP(26 + s); issueH(nb, s); issueC(nb, s); rot(); WAITV(8);
    }
    for (int s = 4; s < 8; s++) {
      LBAR(); LSTAMP(26 + s); issueH(nb, s); issueC(nb, s); rot(); WAITV(8);
    }
    const i32x4 rO = mkrsrc(wout, FH * 512 * 2);
    u32 vO[4];
#pragma unroll
    for (int j = 0; j < 4; j++) {
      int row = 32 * lw + 8 * j + (lane >> 3);
      vO[j] = row * 1024 + j2 * 128 + (((lane & 7) ^ (((row >> 1) & 3) << 1)) << 4);
    }
    const u32 vV = WS_V + ((2 * i2 + (lw >> 1)) * 48 + 4 * (lw & 1)) * 1024 + lane * 16;
    auto issueO = [&](u32 base, int s) {
#pragma unroll
      for (int j = 0; j < 4; j++) dma<0>(base + 16384 + lw * 4096 + j * 1024, vO[j], rO, s * (128 * 1024));
    };
    auto issueV = [&](u32 base, int s) {
#pragma unroll
      for (int j = 0; j < 4; j++) dma<1>(base + lw * 4096 + j * 1024, vV, rWS, s * 8192 + j * 1024);
    };
    {
      LBAR(); LSTAMP(34);
      const u32 vf = WS_F3 + i2 * 256 + m * 16;
      u32x4 f = ld16_issue(vf, rWS);
      issueO(nb, 0);
      WAITV1(4, f);
      if (!flags_ok(f, tlo, thi, true)) spin_flags(vf, rWS, tlo, thi, true);
      LSTAMP(49);
      issueV(nb, 0); rot();
      WAITV(8);
    }
    for (int s = 1; s < 5; s++) {
      LBAR(); LSTAMP(34 + s); issueV(nb, s); issueO(nb, s); rot(); WAITV(8);
    }
    LBAR(); LSTAMP(39);
    if ((lw & 1) == 0) issueV(nb, 5);
    if (lw < 2) issueO(nb, 5);
    if (lw == 0) WAITV(8); else if (lw == 3) WAITV(0); else WAITV(4);
    LBAR(); LSTAMP(40);
    WAITV(0);
    LBAR(); LSTAMP(41);
  } else {
    const int wr = wave >> 1, wc = wave & 1, qq = m >> 2, p = lane & 3;
    const __amdgpu_buffer_rsrc_t rS = __builtin_amdgcn_make_buffer_rsrc(ws, 0, WS_END, 0x00020000);
    const u64 ep = __hip_atomic_load((GAS u64*)(ws + WS_EPOCH), __ATOMIC_RELAXED, __HIP_MEMORY_SCOPE_AGENT);
    const int trow = 64 * i2 + 32 * wr + 8 * (m >> 2) + (m & 3);
    const float bias0 = bf2f(bsp[trow]), bias1 = bf2f(bsp[trow + 4]);
    if (wave == 0) {
      cf lam, coef;
      pole(lre, lim, ldt, 64 * j2 + lane, lam, coef);
      cf t = cmul(lam, lam);
      t = cmul(t, t);
      const cf l8 = cmul(t, t), l16 = cmul(l8, l8), l32 = cmul(l16, l16);
      t = cmul(l32, l32);
      t = cmul(t, t);
      t = cmul(t, t);
      t = cmul(t, t);
      *(LAS f32x4*)(size_t)(sb + L_POW + lane * 16) = f32x4{lam.re, lam.im, 2.f * coef.re, 2.f * coef.im};
      *(LAS f32x4*)(size_t)(sb + L_POW + 1024 + lane * 16) = f32x4{l8.re, l8.im, l16.re, l16.im};
      *(LAS f32x4*)(size_t)(sb + L_POW + 2048 + lane * 16) = f32x4{l32.re, l32.im, t.re, t.im};
    }
    const u64 tag = (((u64)__builtin_amdgcn_readfirstlane((u32)(ep >> 32)) << 32) | (u32)__builtin_amdgcn_readfirstlane((u32)ep)) * 6364136223846793005ull + 1442695040888963407ull;
    const u32 tlo = (u32)tag, thi = (u32)(tag >> 32);
    const int tile0 = 6 * j2 + 3 * wc;
    const int rg = 2 * i2 + wr;

    u32 aR[4], aW[3];
#pragma unroll
    for (int ks = 0; ks < 4; ks++) aR[ks] = sb + m * 256 + (((4 * ks + g) ^ m) << 4);
#pragma unroll
    for (int n = 0; n < 3; n++) aW[n] = sb + 16384 + (8 * g + qq) * 192 + (((3 * wc + n) ^ (g & 1)) << 5) + p * 8;

    bf16x8 xf[2][16];
    bf16x8 wf[2][3];
    auto rdW = [&](bf16x8* w, int kk, int st) {
      const u32 o = (st % 3) * SLOT + (kk & 3) * 6144;
#pragma unroll
      for (int n = 0; n < 3; n++) w[n] = cat(ldstr(aW[n] + o), ldstr(aW[n] + o + 768));
    };
    auto rdX = [&](int kk) {
#pragma unroll
      for (int u = 0; u < 2; u++) xf[u][kk] = lds16(aR[kk & 3] + ((kk >> 2) % 3) * SLOT + (32 * wr + 16 * u) * 256);
    };

    f32x4 z2[2][3] = {};
    __builtin_amdgcn_s_waitcnt(0xC07F);
    LBAR();
    rdX(0); rdW(wf[0], 0, 0);
#pragma unroll
    for (int kk = 0; kk < 16; kk++) {
      if (kk < 15) {
        if ((kk & 3) == 3) LBAR();
        rdX(kk + 1); rdW(wf[(kk + 1) & 1], kk + 1, (kk + 1) >> 2);
      }
      SB();
#pragma unroll
      for (int u = 0; u < 2; u++)
#pragma unroll
        for (int n = 0; n < 3; n++) z2[u][n] = MFMA(xf[u][kk], wf[kk & 1][n], z2[u][n]);
      SB();
    }
    STAMP(0);
    u32 z2p[3][4];
    {
      float s1[8], s2[8];
#pragma unroll
      for (int j = 0; j < 8; j++) { s1[j] = 0.f; s2[j] = 0.f; }
#pragma unroll
      for (int n = 0; n < 3; n++) {
        const bool ok = tile0 + n < FH / 16;
#pragma unroll
        for (int j = 0; j < 8; j++) {
          float v = ok ? gelu(z2[j >> 2][n][j & 3]) : 0.f;
          z2[j >> 2][n][j & 3] = v;
          s1[j] += v;
          s2[j] = __builtin_fmaf(v, v, s2[j]);
        }
#pragma unroll
        for (int k = 0; k < 4; k++) z2p[n][k] = pk2(z2[k >> 1][n][(2 * k) & 3], z2[k >> 1][n][(2 * k + 1) & 3]);
      }
      const float r1 = reduce8(s1, m), r2 = reduce8(s2, m);
      if (!(m & 1))
        __builtin_amdgcn_raw_buffer_store_b128(u32x4{__float_as_uint(r1), tlo, __float_as_uint(r2), thi}, rS,
                                               WS_STAT + ((i2 * 16 + 2 * j2 + wc) * 64 + 32 * wr + 8 * g + (m >> 1)) * 16, 0, 16);
    }
    STAMP(1);
    u32 z1p[2][3][2];
    {
      f32x4 z1[2][3] = {};
      LBAR();
      rdW(wf[0], 0, 4);
#pragma unroll
      for (int kk = 0; kk < 16; kk++) {
        if (kk < 15) {
          if ((kk & 3) == 3) LBAR();
          rdW(wf[(kk + 1) & 1], kk + 1, 4 + ((kk + 1) >> 2));
        }
        SB();
#pragma unroll
        for (int u = 0; u < 2; u++)
#pragma unroll
          for (int n = 0; n < 3; n++) z1[u][n] = MFMA(wf[kk & 1][n], xf[u][kk], z1[u][n]);
        SB();
      }
      STAMP(2);
#pragma unroll
      for (int u = 0; u < 2; u++)
#pragma unroll
        for (int n = 0; n < 3; n++) {
          z1p[u][n][0] = pk2(gelu(z1[u][n][0]), gelu(z1[u][n][1]));
          z1p[u][n][1] = pk2(gelu(z1[u][n][2]), gelu(z1[u][n][3]));
        }
    }
    STAMP(3);
    LBAR();
    STAMP(4);
    {
      const u32 as = sb + L_STAT + (32 * wr + 8 * g) * 8;
      f32x4 st[4];
#pragma unroll
      for (int k = 0; k < 4; k++) st[k] = *(const LAS f32x4*)(size_t)(as + k * 16);
#pragma unroll
      for (int n = 0; n < 3; n++) {
        const bool ok = tile0 + n < FH / 16;
        u32x4 o;
#pragma unroll
        for (int k = 0; k < 4; k++) {
          float a = __builtin_fmaf(lo16(z2p[n][k]), st[k][0], st[k][1]);
          float b = __builtin_fmaf(hi16(z2p[n][k]), st[k][2], st[k][3]);
          o[k] = ok ? pk2(a, b) : 0u;
        }
        __builtin_amdgcn_raw_buffer_store_b128(o, rS, WS_Z + (rg * 48 + tile0 + n) * 1024 + lane * 16, 0, 16);
      }
    }
    STAMP(5);
    f32x4 ur[2][2] = {}, ui[2][2] = {};
    {
      bf16x8 bf[2][4];
      auto rdB = [&](bf16x8* b, int kk) {
        const u32 o = ((8 + (kk >> 2)) % 3) * SLOT + 32 * wc * 256;
#pragma unroll
        for (int c = 0; c < 4; c++) b[c] = lds16(aR[kk & 3] + o + (c >> 1) * 16384 + (c & 1) * 4096);
      };
      rdB(bf[0], 0);
#pragma unroll
      for (int kk = 0; kk < 16; kk++) {
        if (kk < 15) {
          if ((kk & 3) == 3) LBAR();
          rdB(bf[(kk + 1) & 1], kk + 1);
        }
        SB();
#pragma unroll
        for (int u = 0; u < 2; u++)
#pragma unroll
          for (int a = 0; a < 2; a++) {
            ur[u][a] = MFMA(xf[u][kk], bf[kk & 1][a], ur[u][a]);
            ui[u][a] = MFMA(xf[u][kk], bf[kk & 1][2 + a], ui[u][a]);
          }
        SB();
        if (kk == 2) put_flag(ws, WS_F1 + ((j2 * 32 + i2) * 4 + wave) * 8, tag, lane);
      }
    }
    STAMP(6);
    cf pre[2];
#pragma unroll
    for (int a = 0; a < 2; a++) {
      const u32 ap = sb + L_POW + (32 * wc + 16 * a + m) * 16;
      const f32x4 t0 = *(const LAS f32x4*)(size_t)ap, t1 = *(const LAS f32x4*)(size_t)(ap + 1024);
      const cf lam = {t0[0], t0[1]}, l8 = {t1[0], t1[1]}, l16 = {t1[2], t1[3]};
      cf e = {ur[0][a][0], ui[0][a][0]};
#pragma unroll
      for (int j = 1; j < 8; j++) e = cfma(lam, e, cf{ur[j >> 2][a][j & 3], ui[j >> 2][a][j & 3]});
      cf o = {__shfl_up(e.re, 16), __shfl_up(e.im, 16)};
      if (g >= 1) e = cfma(l8, o, e);
      o = {__shfl_up(e.re, 32), __shfl_up(e.im, 32)};
      if (g >= 2) e = cfma(l16, o, e);
      o = {__shfl_up(e.re, 16), __shfl_up(e.im, 16)};
      pre[a] = g ? o : cf{0.f, 0.f};
      if (g == 3)
        __builtin_amdgcn_raw_buffer_store_b64(u32x2{__float_as_uint(e.re), __float_as_uint(e.im)}, rS,
                                              WS_SEG + (j2 * 64 + rg) * 512 + (32 * wc + 16 * a + m) * 8, 0, 16);
    }
    STAMP(7);
    f32x4 ga[2][3] = {};
    {
      const u32 aZ = sb + 16384 + 3 * wc * 1024 + lane * 16;
      bf16x8 ca[3], cb[2], na[3], nbf[2];
      auto rd = [&](bf16x8* a, bf16x8* b, u32 so, int ks) {
        b[0] = lds16(aR[ks] + so + 32 * wr * 256);
#pragma unroll
        for (int n = 0; n < 3; n++) a[n] = lds16(aZ + so + (ks * 6 + n) * 1024);
        b[1] = lds16(aR[ks] + so + (32 * wr + 16) * 256);
      };
      auto mm = [&](bf16x8* a, bf16x8* b) {
#pragma unroll
        for (int u = 0; u < 2; u++)
#pragma unroll
          for (int n = 0; n < 3; n++) ga[u][n] = MFMA(a[n], b[u], ga[u][n]);
      };
      u32 so = 0;
      LBAR();
      STAMP(8);
      rd(ca, cb, so, 0);
      for (int s = 0; s < 16; s++) {
        rd(na, nbf, so, 1); SB(); mm(ca, cb); SB();
        rd(ca, cb, so, 2); SB(); mm(na, nbf); SB();
        rd(na, nbf, so, 3); SB(); mm(ca, cb); SB();
        if (s == 0) put_flag(ws, WS_FW + ((j2 * 32 + i2) * 4 + wave) * 8, tag, lane);
        if (s == 9) put_flag(ws, WS_F2 + ((i2 * 8 + j2) * 4 + wave) * 8, tag, lane);
        if (s == 8) {
#pragma unroll
          for (int a = 0; a < 2; a++) {
            const f32x2 cw = *(const LAS f32x2*)(size_t)(sb + L_CARRY + wr * 512 + (32 * wc + 16 * a + m) * 8);
            const u32 ap = sb + L_POW + (32 * wc + 16 * a + m) * 16;
            const f32x4 t0 = *(const LAS f32x4*)(size_t)ap, t1 = *(const LAS f32x4*)(size_t)(ap + 1024);
            const cf lam = {t0[0], t0[1]}, c2 = {t0[2], t0[3]};
            cf lq = (g & 1) ? cf{t1[0], t1[1]} : cf{1.f, 0.f};
            if (g & 2) lq = cmul(lq, cf{t1[2], t1[3]});
            cf h = cfma(lq, cf{cw[0], cw[1]}, pre[a]);
            float hr[8], hi[8];
#pragma unroll
            for (int j = 0; j < 8; j++) {
              h = cfma(lam, h, cf{ur[j >> 2][a][j & 3], ui[j >> 2][a][j & 3]});
              hr[j] = c2.re * h.re - c2.im * h.im;
              hi[j] = -(c2.re * h.im + c2.im * h.re);
            }
            const bool sw = m & 8;
            const u32 r0 = pk2(hr[0], hr[1]), r1 = pk2(hr[2], hr[3]), r2 = pk2(hr[4], hr[5]), r3 = pk2(hr[6], hr[7]);
            const u32 i0 = pk2(hi[0], hi[1]), i1 = pk2(hi[2], hi[3]), i2_ = pk2(hi[4], hi[5]), i3 = pk2(hi[6], hi[7]);
            const u32 off = WS_H + (rg * 32 + 4 * j2 + 2 * wc + a) * 1024 + (m * 4 + g) * 16;
            __builtin_amdgcn_raw_buffer_store_b128(sw ? u32x4{r2, r3, r0, r1} : u32x4{r0, r1, r2, r3}, rS, off, 0, 16);
            __builtin_amdgcn_raw_buffer_store_b128(sw ? u32x4{i2_, i3, i0, i1} : u32x4{i0, i1, i2_, i3}, rS, off + 64 * 32 * 1024, 0, 16);
          }
        }
        so += SLOT;
        if (so == 3 * SLOT) so = 0;
        if (s < 15) { LBAR(); rd(ca, cb, so, 0); }
        SB(); mm(na, nbf); SB();
      }
    }
    STAMP(9);
#pragma unroll
    for (int n = 0; n < 3; n++) {
      if (tile0 + n < 44) {
        const bool ok = tile0 + n < FH / 16;
        u32 h[2][2];
#pragma unroll
        for (int u = 0; u < 2; u++) {
          const float b = u ? bias1 : bias0;
          h[u][0] = pk2(lo16(z1p[u][n][0]) * (ga[u][n][0] + b), hi16(z1p[u][n][0]) * (ga[u][n][1] + b));
          h[u][1] = pk2(lo16(z1p[u][n][1]) * (ga[u][n][2] + b), hi16(z1p[u][n][1]) * (ga[u][n][3] + b));
        }
        u32x4 o = (g & 1) ? u32x4{h[1][0], h[1][1], h[0][0], h[0][1]} : u32x4{h[0][0], h[0][1], h[1][0], h[1][1]};
        if (!ok) o = u32x4{0u, 0u, 0u, 0u};
        __builtin_amdgcn_raw_buffer_store_b128(o, rS, WS_V + (rg * 48 + tile0 + n) * 1024 + lane * 16, 0, 16);
      }
    }
    STAMP(10);
    f32x4 ya[2][2] = {}, yb[2][2] = {};
    const int dcol = 64 * j2 + 32 * wc + 4 * g;
    u32x2 xe[2][2], de[2];
    {
      u32 aH[2];
#pragma unroll
      for (int u = 0; u < 2; u++) aH[u] = sb + (wr * 8 + (g >> 1)) * 1024 + (8 * (g & 1) + qq) * 64 + p * 16 + ((u ^ (g & 1)) << 3);
      bf16x8 ca[2], cb[2], na[2], nbf[2];
      auto rd = [&](bf16x8* a, bf16x8* b, u32 so, int ks) {
#pragma unroll
        for (int u = 0; u < 2; u++) {
          b[u] = cat(ldstr(aH[u] + so + ks * 2048), ldstr(aH[u] + so + ks * 2048 + 256));
          a[u] = lds16(aR[ks] + so + 16384 + (32 * wc + 16 * u) * 256);
        }
      };
      auto mm = [&](bf16x8* a, bf16x8* b) {
#pragma unroll
        for (int u = 0; u < 2; u++)
#pragma unroll
          for (int n = 0; n < 2; n++) ya[u][n] = MFMA(a[n], b[u], ya[u][n]);
      };
      u32 so = SLOT;
      LBAR();
      rd(ca, cb, so, 0);
      for (int s = 0; s < 8; s++) {
        rd(na, nbf, so, 1); SB(); mm(ca, cb); SB();
        rd(ca, cb, so, 2); SB(); mm(na, nbf); SB();
        rd(na, nbf, so, 3); SB(); mm(ca, cb); SB();
        if (s == 0) put_flag(ws, WS_F3 + ((i2 * 8 + j2) * 4 + wave) * 8, tag, lane);
        so += SLOT;
        if (so == 3 * SLOT) so = 0;
        if (s < 7) { LBAR(); rd(ca, cb, so, 0); }
        SB(); mm(na, nbf); SB();
      }
    }
    STAMP(11);
#pragma unroll
    for (int n = 0; n < 2; n++) {
      de[n] = *(const u32x2*)(dsk + dcol + 16 * n);
#pragma unroll
      for (int u = 0; u < 2; u++) xe[u][n] = *(const u32x2*)(x + (trow + 4 * u) * 512 + dcol + 16 * n);
    }
    {
      u32 aV[2], aO[2];
#pragma unroll
      for (int u = 0; u < 2; u++) {
        aV[u] = sb + wr * 8192 + lane * 16 + ((u ^ (g & 1)) << 3);
        aO[u] = sb + 16384 + (4 * g + qq) * 128 + (((2 * wc + u) ^ (2 * (g & 1) + (qq >> 1))) << 5) + p * 8;
      }
      bf16x8 ca[2], cb[2], na[2], nbf[2];
      auto rd = [&](bf16x8* a, bf16x8* b, u32 so, int ks) {
#pragma unroll
        for (int u = 0; u < 2; u++) {
          b[u] = cat(lds8(aV[u] + so + ks * 2048), lds8(aV[u] + so + ks * 2048 + 1024));
          a[u] = cat(ldstr(aO[u] + so + ks * 4096), ldstr(aO[u] + so + ks * 4096 + 2048));
        }
      };
      auto mm = [&](bf16x8* a, bf16x8* b) {
#pragma unroll
        for (int u = 0; u < 2; u++)
#pragma unroll
          for (int n = 0; n < 2; n++) yb[u][n] = MFMA(a[n], b[u], yb[u][n]);
      };
      u32 so = 0;
      LBAR();
      rd(ca, cb, so, 0);
      for (int s = 0; s < 5; s++) {
        rd(na, nbf, so, 1); SB(); mm(ca, cb); SB();
        rd(ca, cb, so, 2); SB(); mm(na, nbf); SB();
        rd(na, nbf, so, 3); SB(); mm(ca, cb); SB();
        so += SLOT;
        if (so == 3 * SLOT) so = 0;
        LBAR(); rd(ca, cb, so, 0);
        SB(); mm(na, nbf); SB();
      }
      rd(na, nbf, so, 1); SB(); mm(ca, cb); SB();
      mm(na, nbf);
    }
    STAMP(12);
#pragma unroll
    for (int u = 0; u < 2; u++) {
      const float gt = *(const LAS float*)(size_t)(sb + L_GATE + (32 * wr + 16 * u + m) * 4);
#pragma unroll
      for (int n = 0; n < 2; n++) {
        f32x4 o;
        o[0] = __builtin_fmaf(lo16(xe[u][n][0]), lo16(de[n][0]), ya[u][n][0]);
        o[1] = __builtin_fmaf(hi16(xe[u][n][0]), hi16(de[n][0]), ya[u][n][1]);
        o[2] = __builtin_fmaf(lo16(xe[u][n][1]), lo16(de[n][1]), ya[u][n][2]);
        o[3] = __builtin_fmaf(hi16(xe[u][n][1]), hi16(de[n][1]), ya[u][n][3]);
#pragma unroll
        for (int r = 0; r < 4; r++) o[r] = gt * o[r] + (1.0f - gt) * yb[u][n][r];
        *(f32x4*)(out + (trow + 4 * u) * 512 + dcol + 16 * n) = o;
      }
    }
    STAMP(13);
  }
}
}

extern "C" void kernel_launch(void* const* d_in, const int* in_sizes, int n_in, void* d_out, int out_size, void* d_ws, size_t ws_size,
                              hipStream_t stream) {
  (void)hipFuncSetAttribute((const void*)fused_kernel, hipFuncAttributeMaxDynamicSharedMemorySize, LDS_BYTES);
  fused_kernel<<<256, 512, LDS_BYTES, stream>>>((const u16*)d_in[0], (const u16*)d_in[1], (const u16*)d_in[2], (const u16*)d_in[3],
                                                (const u16*)d_in[4], (const u16*)d_in[5], (const u16*)d_in[6], (const u16*)d_in[7],
                                                (const u16*)d_in[8], (const u16*)d_in[10], (const u16*)d_in[11], (const u16*)d_in[12],
                                                (const u16*)d_in[13], (const u16*)d_in[14], (float*)d_out, (char*)d_ws);
}
```

```cpp
#include <hip/hip_runtime.h>
#include <stdint.h>

namespace {
typedef unsigned long long u64;
typedef unsigned short u16;
typedef unsigned u32;
typedef short bf16x8 __attribute__((ext_vector_type(8)));
typedef short s16x4 __attribute__((ext_vector_type(4)));
typedef float f32x4 __attribute__((ext_vector_type(4)));
typedef float f32x2 __attribute__((ext_vector_type(2)));
typedef __bf16 bf2 __attribute__((ext_vector_type(2)));
typedef int i32x4 __attribute__((ext_vector_type(4)));
typedef u32 u32x4 __attribute__((ext_vector_type(4)));
typedef u32 u32x2 __attribute__((ext_vector_type(2)));
#define LAS __attribute__((address_space(3)))
#define GAS __attribute__((address_space(1)))
#define DI __device__ __forceinline__

constexpr int FH = 688;
constexpr u32 SLOT = 40960;
constexpr u32 L_GW = 3 * SLOT, L_STAT = L_GW + 1024, L_GATE = L_STAT + 512, L_CARRY = L_GATE + 256, L_W0 = L_CARRY + 512;
constexpr u32 L_POW = L_W0 + 512;
constexpr u32 L_DUMMY = L_POW + 4096;
constexpr u32 LDS_BYTES = L_DUMMY + 256;

constexpr u32 WS_EPOCH = 0;
constexpr u32 WS_F1 = 4096, WS_F2 = WS_F1 + 8192, WS_F3 = WS_F2 + 8192;
constexpr u32 WS_STAT = 65536;
constexpr u32 WS_SEG = WS_STAT + 32 * 16 * 64 * 16;
constexpr u32 WS_Z = 1u << 20;
constexpr u32 WS_H = WS_Z + 64 * 48 * 1024;
constexpr u32 WS_V = WS_H + 2 * 64 * 32 * 1024;
constexpr u32 WS_END = WS_V + 64 * 48 * 1024;
constexpr u32 WS_STAMP = 16u << 20;
constexpr u64 SPIN_TICKS = 2000000;

constexpr u32 BIAS = 4096;
constexpr u32 OOB = 0x80000000u;
DI i32x4 mkrsrc(const void* p, u32 bytes) {
  u64 a = (u64)p - BIAS;
  i32x4 r;
  r[0] = __builtin_amdgcn_readfirstlane((u32)a);
  r[1] = __builtin_amdgcn_readfirstlane((u32)(a >> 32) & 0xffff);
  r[2] = bytes + BIAS;
  r[3] = 0x00020000;
  return r;
}
#define M0SET "s_mov_b32 m0, %0\n\ts_nop 0\n\t"
#define BL(v, o, f) "buffer_load_dwordx4 %" #v ", %1, %2 offen offset:" #o f " lds\n\t"
DI void dma1(u32 lds, i32x4 rs, u32 soff, u32 v0) {
  asm volatile(M0SET BL(3, 0, "")::"s"(lds), "s"(rs), "s"(soff), "v"(v0) : "memory");
}
DI void dma2(u32 lds, i32x4 rs, u32 soff, const u32* v) {
  asm volatile(M0SET BL(3, 0, "") BL(4, 1024, "")::"s"(lds), "s"(rs), "s"(soff), "v"(v[0]), "v"(v[1]) : "memory");
}
DI void dma4(u32 lds, i32x4 rs, u32 soff, const u32* v) {
  asm volatile(M0SET BL(3, 0, "") BL(4, 1024, "") BL(5, 2048, "") BL(6, 3072, "")::"s"(lds), "s"(rs), "s"(soff), "v"(v[0]), "v"(v[1]), "v"(v[2]), "v"(v[3]) : "memory");
}
DI void dma2c(u32 lds, i32x4 rs, u32 soff, u32 v0) {
  asm volatile(M0SET BL(3, 0, " sc1") BL(3, 1024, " sc1")::"s"(lds), "s"(rs), "s"(soff), "v"(v0) : "memory");
}
DI void dma4c(u32 lds, i32x4 rs, u32 soff, u32 v0) {
  asm volatile(M0SET BL(3, 0, " sc1") BL(3, 1024, " sc1") BL(3, 2048, " sc1") BL(3, 3072, " sc1")::"s"(lds), "s"(rs), "s"(soff), "v"(v0) : "memory");
}
DI void touch(u32 lds, const void* p) {
  asm volatile("s_mov_b32 m0, %0\n\ts_nop 0\n\tglobal_load_lds_dword %1, off" ::"s"(lds), "v"(p) : "memory");
}
DI u32x4 ld16_issue(u32 voff, i32x4 rs) {
  u32x4 r;
  asm volatile("buffer_load_dwordx4 %0, %1, %2, 0 offen sc1" : "=v"(r) : "v"(voff), "s"(rs) : "memory");
  return r;
}
DI u32x2 ld8_issue(u32 voff, i32x4 rs) {
  u32x2 r;
  asm volatile("buffer_load_dwordx2 %0, %1, %2, 0 offen sc1" : "=v"(r) : "v"(voff), "s"(rs) : "memory");
  return r;
}
#define WAITV(n) asm volatile("s_waitcnt vmcnt(%0)" ::"n"(n) : "memory")
#define WAITV1(n, a) asm volatile("s_waitcnt vmcnt(%1)" : "+v"(a) : "n"(n) : "memory")
#define WAITV4(n, a, b, c, d) asm volatile("s_waitcnt vmcnt(%4)" : "+v"(a), "+v"(b), "+v"(c), "+v"(d) : "n"(n) : "memory")
#define WAITV8(a) asm volatile("s_waitcnt vmcnt(0)" : "+v"(a[0]), "+v"(a[1]), "+v"(a[2]), "+v"(a[3]), "+v"(a[4]), "+v"(a[5]), "+v"(a[6]), "+v"(a[7])::"memory")
#define LBAR() asm volatile("s_waitcnt lgkmcnt(0)\n\ts_barrier" ::: "memory")
#define PIN(a) asm volatile("" : "+v"(a))
#define MF(c, a, b) asm volatile("v_mfma_f32_16x16x32_bf16 %0, %1, %2, %0" : "+v"(c) : "v"(a), "v"(b))
#define MF0(c, a, b) asm volatile("v_mfma_f32_16x16x32_bf16 %0, %1, %2, 0" : "=&v"(c) : "v"(a), "v"(b))
#define MFK(first, c, a, b) do { if (first) MF0(c, a, b); else MF(c, a, b); } while (0)
#define MFDONE4(a, b, c, d) asm volatile("s_nop 7\n\ts_nop 1" : "+v"(a), "+v"(b), "+v"(c), "+v"(d))
#define MFDONE6(a, b, c, d, e, f) asm volatile("s_nop 7\n\ts_nop 1" : "+v"(a), "+v"(b), "+v"(c), "+v"(d), "+v"(e), "+v"(f))

DI bf16x8 lds16(u32 a) { return *(const LAS bf16x8*)(size_t)a; }
DI f32x4 ldsf4(u32 a) { return *(const LAS f32x4*)(size_t)a; }
DI f32x2 ldsf2(u32 a) { return *(const LAS f32x2*)(size_t)a; }
DI s16x4 lds8(u32 a) { return *(const LAS s16x4*)(size_t)a; }
DI s16x4 ldstr(u32 a) { return __builtin_amdgcn_ds_read_tr16_b64_v4i16((LAS s16x4*)(size_t)a); }
DI bf16x8 cat(s16x4 lo, s16x4 hi) { return __builtin_shufflevector(lo, hi, 0, 1, 2, 3, 4, 5, 6, 7); }
DI bf16x8 ldtr2(u32 a, u32 d) { return cat(ldstr(a), ldstr(a + d)); }
DI float bf2f(u16 v) { return __uint_as_float((u32)v << 16); }
DI u32 pk(f32x2 v) { return __builtin_bit_cast(u32, __builtin_convertvector(v, bf2)); }
DI f32x2 unpk(u32 v) { return f32x2{__uint_as_float(v << 16), __uint_as_float(v & 0xffff0000u)}; }
DI f32x2 fma2(f32x2 a, f32x2 b, f32x2 c) { return __builtin_elementwise_fma(a, b, c); }
DI f32x2 sp(float a) { return f32x2{a, a}; }
DI f32x2 half(f32x4 v, int h) { return h ? v.zw : v.xy; }
DI f32x2 gelu2(f32x2 x) {
  f32x2 a = x * fma2(sp(-0.1029432397f), x * x, sp(-2.302208198f));
  f32x2 e = f32x2{__builtin_amdgcn_exp2f(a[0]), __builtin_amdgcn_exp2f(a[1])} + 1.0f;
  return x * f32x2{__builtin_amdgcn_rcpf(e[0]), __builtin_amdgcn_rcpf(e[1])};
}
struct cf { float re, im; };
DI cf cmul(cf a, cf b) { return {a.re * b.re - a.im * b.im, a.re * b.im + a.im * b.re}; }
DI cf cfma(cf a, cf b, cf c) {
  return {__builtin_fmaf(a.re, b.re, __builtin_fmaf(-a.im, b.im, c.re)), __builtin_fmaf(a.re, b.im, __builtin_fmaf(a.im, b.re, c.im))};
}
DI void sincos_cw(float x, float& s, float& c) {
  float k = __builtin_rintf(x * 0.6366197724f);
  float r = __builtin_fmaf(k, -1.57079637050628662109375f, x);
  r = __builtin_fmaf(k, 4.37113900018624283e-8f, r);
  float r2 = r * r;
  float ps = __builtin_fmaf(r2, __builtin_fmaf(r2, __builtin_fmaf(r2, 2.7557314297e-06f, -1.9841270114e-04f), 8.3333337680e-03f), -1.6666667163e-01f);
  float sr = __builtin_fmaf(r * r2, ps, r);
  float pc = __builtin_fmaf(r2, __builtin_fmaf(r2, __builtin_fmaf(r2, -2.7557314297e-07f, 2.4801587642e-05f), -1.3888889225e-03f), 4.1666667908e-02f);
  float cr = __builtin_fmaf(r2 * r2, pc, __builtin_fmaf(r2, -0.5f, 1.0f));
  int q = (int)k;
  float ss = (q & 1) ? cr : sr, cc = (q & 1) ? sr : cr;
  s = (q & 2) ? -ss : ss;
  c = ((q + 1) & 2) ? -cc : cc;
}
template <int CTRL>
DI float dpp(float v) {
  return __int_as_float(__builtin_amdgcn_update_dpp(0, __float_as_int(v), CTRL, 0xf, 0xf, true));
}
DI float reduce8(const f32x2* v, int m) {
  float a[4], b[2];
  const bool h3 = m & 8, h2 = m & 4, h1 = m & 2;
#pragma unroll
  for (int k = 0; k < 4; k++) {
    const float lo = v[k >> 1][k & 1], hi = v[2 + (k >> 1)][k & 1];
    a[k] = (h3 ? hi : lo) + dpp<0x140>(h3 ? lo : hi);
  }
#pragma unroll
  for (int k = 0; k < 2; k++) b[k] = (h2 ? a[k + 2] : a[k]) + dpp<0x141>(h2 ? a[k] : a[k + 2]);
  float c = (h1 ? b[1] : b[0]) + dpp<0x4E>(h1 ? b[0] : b[1]);
  return c + dpp<0xB1>(c);
}
DI void rows4(float v, float* o) {
  auto s = __builtin_amdgcn_permlane16_swap(__float_as_uint(v), __float_as_uint(v), false, false);
  auto e = __builtin_amdgcn_permlane32_swap(s[0], s[0], false, false);
  auto d = __builtin_amdgcn_permlane32_swap(s[1], s[1], false, false);
  o[0] = __uint_as_float(e[0]); o[1] = __uint_as_float(d[0]); o[2] = __uint_as_float(e[1]); o[3] = __uint_as_float(d[1]);
}
DI float halves(float v) {
  auto s = __builtin_amdgcn_permlane32_swap(__float_as_uint(v), __float_as_uint(v), false, false);
  return __uint_as_float(s[0]) + __uint_as_float(s[1]);
}
DI bool none(u32 d) { PIN(d); return __builtin_amdgcn_ballot_w64(d != 0) == 0; }
DI u32 tagdiff(u32x4 v, u32 lo, u32 hi) { return (v[1] ^ lo) | (v[3] ^ hi); }
DI bool flags_ok(u32x4 v, u32 lo, u32 hi, bool need) { return none(need ? (v[0] ^ lo) | (v[1] ^ hi) | (v[2] ^ lo) | (v[3] ^ hi) : 0u); }
DI void spin_flags(u32 voff, i32x4 rs, u32 lo, u32 hi, bool need) {
  const u64 t0 = __builtin_amdgcn_s_memrealtime();
  for (;;) {
    u32x4 v = ld16_issue(voff, rs);
    WAITV1(0, v);
    if (flags_ok(v, lo, hi, need) || __builtin_amdgcn_s_memrealtime() - t0 > SPIN_TICKS) break;
    __builtin_amdgcn_s_sleep(2);
  }
}
DI void put_flag(char* ws, u32 off, u64 tag, int lane) {
  WAITV(0);
  if (lane == 0) __hip_atomic_store((GAS u64*)(ws + off), tag, __ATOMIC_RELAXED, __HIP_MEMORY_SCOPE_AGENT);
}
DI u64 next_tag(u32 lo, u32 hi) {
  return (((u64)(u32)__builtin_amdgcn_readfirstlane(hi) << 32) | (u32)__builtin_amdgcn_readfirstlane(lo)) * 6364136223846793005ull + 1442695040888963407ull;
}
#define STAT_BAR 7
#define SLP6 3
#define SLP7 7
#define KQ ((xcd & 3) * 4)
#define PRIO_D 0
#define PRIO_A 3
#define PRIO_C 2
#define LOADER_PRIO __builtin_amdgcn_s_setprio(PRIO_D)
#define MFMA_PRIO __builtin_amdgcn_s_setprio(PRIO_C)
DI void try_flag(bool& done, bool force, char* ws, u32 off, u64 tag, int lane) {
  if (!done) {
    u32 st;
    asm volatile("s_getreg_b32 %0, hwreg(HW_REG_IB_STS)" : "=s"(st));
    if (force || (st & 0x00C0000Fu) == 0) { put_flag(ws, off, tag, lane); done = true; }
  }
}
#define STAMP(k)
#define LSTAMP(k) STAMP(k)

__global__ __launch_bounds__(512, 2) void fused_kernel(const u16* x, const u16* lre, const u16* lim, const u16* bre, const u16* bim,
                                                       const u16* cre, const u16* cim, const u16* ldt, const u16* dsk, const u16* win,
                                                       const u16* wsp, const u16* bsp, const u16* wout, const u16* gw, float* out, char* ws) {
  extern __shared__ __attribute__((aligned(16))) char smem[];
  const int tid = threadIdx.x, wave = __builtin_amdgcn_readfirstlane(tid >> 6), lane = tid & 63;
  const int xcd = blockIdx.x & 7, idx = blockIdx.x >> 3;
  const int i2 = (xcd & 3) * 8 + (idx & 7), j2 = (xcd >> 2) * 4 + (idx >> 3);
  const bool narrow = (j2 & 3) == 3;
  const int tb = 6 * j2 - 2 * (j2 >> 2);
  const u32 sb = (u32)(size_t)(LAS char*)smem;
  const int m = lane & 15, g = lane >> 4;

  u64 code;
  int code_len;
  asm volatile("s_getpc_b64 %0\n.Lfused_begin:\n\ts_mov_b32 %1, .Lfused_end_c-.Lfused_begin\n\ts_max_i32 %1, %1, .Lfused_end_l-.Lfused_begin" : "=s"(code), "=s"(code_len)::"scc");

  const int kx = (i2 + j2) & 3;
  const int kq = KQ;

  if (wave < 2) {
    const int dw = wave;
    LOADER_PRIO;
    const i32x4 rX = mkrsrc(x, 2048 * 512 * 2), rW = mkrsrc(win, 512 * 1376 * 2), rWS = mkrsrc(ws, WS_END);
    const int rowA = 64 * i2 + 32 * dw + g;
    u32 vX[4], vW[6];
#pragma unroll
    for (int j = 0; j < 4; j++) vX[j] = (rowA + 8 * j) * 1024 + ((m ^ (4 * j + g)) << 4) + BIAS - 1024 * j;
    u32 nb = sb;
    auto rot = [&]() { nb += SLOT; if (nb == sb + 3 * SLOT) nb = sb; };
    auto rows = [&](u32 lds, i32x4 rs, u32 soff, u32 second, const u32* v) { dma4(lds, rs, soff, v); dma4(lds + 4096, rs, soff + second, v); };
    auto issueX = [&](int s) { rows(nb + dw * 8192, rX, (s ^ kx) * 256, 4 * 1024, vX); };
    issueX(0);
#pragma unroll
    for (int n = 0; n < 6; n++) {
      const int c = n * 64 + lane, k = c / 12, cc = c - 12 * k;
      const int o = max((64 * dw + k) * 2752 + 32 * tb - 32 + ((cc ^ (((k >> 3) & 1) << 1)) << 4), 0);
      vW[n] = narrow && cc >= 8 ? OOB : o + BIAS - 1024 * (n & 3);
    }
    auto issueW = [&](int s, u32 col) {
#pragma unroll
      for (int q = 0; q < 2; q++) {
        const u32 l = nb + 16384 + dw * 12288 + q * 6144, o = (s ^ kx) * (128 * 2752) + q * (32 * 2752) + col;
        dma4(l, rW, o, vW);
        dma2(l + 4096, rW, o, vW + 4);
      }
    };
    issueW(0, 2 * FH);
    rot();
    issueX(1);
    issueW(1, 2 * FH);
    rot();
    const i32x4 rB = mkrsrc(dw ? bim : bre, 512 * 512 * 2), rA = mkrsrc(wsp, 2048 * 2048 * 2);
    u32 vB[4], vA[4];
#pragma unroll
    for (int j = 0; j < 4; j++) {
      vB[j] = (64 * j2 + 4 * j + g) * 1024 + ((m ^ (4 * j + g)) << 4) + BIAS - 1024 * j;
      vA[j] = (rowA + 8 * j) * 4096 + ((m ^ (4 * j + g)) << 4) + BIAS - 1024 * j;
      PIN(vB[j]); PIN(vA[j]);
    }
    u32 vZ = BIAS + WS_Z + (2 * dw * 48 + tb) * 1024 + lane * 16, vZ2 = narrow ? OOB : vZ;
    PIN(vZ); PIN(vZ2);
    WAITV(20);
#pragma nounroll
    for (int s = 0; s < 2; s++) { LBAR(); LSTAMP(s); issueX(s + 2); issueW(s + 2, 2 * FH); rot(); WAITV(20); }
#pragma nounroll
    for (int s = 0; s < 4; s++) {
      LBAR(); LSTAMP(2 + s);
      rows(nb + dw * 16384, rB, (s ^ kx) * 256, 16 * 1024, vB);
      rows(nb + dw * 16384 + 8192, rB, (s ^ kx) * 256 + 32 * 1024, 16 * 1024, vB);
      rot(); WAITV(16);
    }
    auto issueA = [&](int s) { rows(nb + dw * 8192, rA, (((s + kq) & 15) ^ kx) * 256, 4 * 4096, vA); };
    auto issueZ = [&](int s) {
#pragma unroll
      for (int q = 0; q < 2; q++) {
        const u32 l = nb + 16384 + dw * 12288 + q * 6144, o = (((s + kq) & 15) ^ kx) * (4 * 48 * 1024) + q * (48 * 1024);
        dma4c(l, rWS, o, vZ);
        dma2c(l + 4096, rWS, o + 4096, vZ2);
      }
    };
    LBAR(); LSTAMP(6); __builtin_amdgcn_s_sleep(SLP6); issueW(0, 0); issueA(2); rot(); WAITV(20);
    LBAR(); LSTAMP(7); __builtin_amdgcn_s_sleep(SLP7); issueW(1, 0); issueA(0); rot(); WAITV(28);
    LBAR(); LSTAMP(8); issueW(2, 0); issueA(1); rot(); WAITV(28);
    LBAR(); LSTAMP(9); issueW(3, 0); rot();
    const i32x4 rC0 = mkrsrc(cre, 512 * 512 * 2), rC1 = mkrsrc(cim, 512 * 512 * 2), rO = mkrsrc(wout, FH * 512 * 2);
    u32 vC[4], vO[4], vE[4];
#pragma unroll
    for (int j = 0; j < 4; j++) {
      vC[j] = (64 * j2 + 32 * dw + 4 * j + g) * 1024 + ((m ^ (4 * j + g)) << 4) + BIAS - 1024 * j;
      const int row = 64 * dw + 8 * j + (lane >> 3);
      vO[j] = row * 1024 + j2 * 128 + (((lane & 7) ^ (((row >> 1) & 3) << 1)) << 4) + BIAS - 1024 * j;
      const int mm = 8 * (j & 1) + (lane >> 3);
      vE[j] = (64 * i2 + 32 * dw + 4 * (j >> 1) + 8 * (mm >> 2) + (mm & 3)) * 1024 + j2 * 128 + (((lane & 7) ^ (mm >> 1)) << 4) + BIAS - 1024 * j;
      PIN(vC[j]); PIN(vO[j]); PIN(vE[j]);
    }
    u32 vH = BIAS + WS_H + (2 * i2 + dw) * 32 * 1024 + lane * 16;
    u32 vV = BIAS + WS_V + ((2 * i2 + dw) * 48 + 1) * 1024 + lane * 16;
    PIN(vH); PIN(vV);
    WAITV(20);
#pragma nounroll
    for (int s = 0; s < 3; s++) { LBAR(); LSTAMP(10 + s); issueZ(s); rot(); WAITV(12); }
#pragma nounroll
    for (int s = 3; s < 16; s++) { LBAR(); LSTAMP(10 + s); issueZ(s); issueA(s); rot(); WAITV(20); }
    auto frags = [&](u32 soff, u32 v) { dma4c(nb + dw * 8192, rWS, soff, v); dma4c(nb + dw * 8192 + 4096, rWS, soff + 4096, v); };
#pragma nounroll
    for (int s = 0; s < 4; s++) {
      LBAR(); LSTAMP(26 + s);
      if (s == 2) dma4(sb + 32768 + dw * 4096, rX, 0, vE);
      frags((s ^ kx) * 8192, vH);
      rows(nb + 16384 + dw * 8192, rC0, (s ^ kx) * 256, 16 * 1024, vC);
      rot(); WAITV(16);
    }
#pragma nounroll
    for (int s = 0; s < 4; s++) {
      LBAR(); LSTAMP(30 + s);
      frags(64 * 32 * 1024 + (s ^ kx) * 8192, vH);
      rows(nb + 16384 + dw * 8192, rC1, (s ^ kx) * 256, 16 * 1024, vC);
      rot(); WAITV(16);
    }
#pragma nounroll
    for (int s = 0; s < 5; s++) {
      const int t = s < 4 ? s ^ kx : s;
      LBAR(); LSTAMP(34 + s);
      frags(t * 8192, vV);
      rows(nb + 16384 + dw * 8192, rO, t * (128 * 1024), 32 * 1024, vO);
      rot(); WAITV(16);
    }
    LBAR(); LSTAMP(39);
    dma4c(nb + dw * 8192, rWS, 5 * 8192, vV);
    dma4(nb + 16384 + dw * 4096, rO, 5 * (128 * 1024) - dw * (32 * 1024), vO);
    WAITV(8);
    LBAR(); LSTAMP(40);
    WAITV(0);
    LBAR(); LSTAMP(41);
    asm volatile(".Lfused_end_l:");
    __builtin_amdgcn_endpgm();
  } else if (wave < 4) {
    const int aw = wave - 2, r32 = lane & 31, hf = lane >> 5;
    __builtin_amdgcn_s_setprio(PRIO_A);
    const i32x4 rWS = mkrsrc(ws, WS_END);
    u32x2 ep = ld8_issue(WS_EPOCH + BIAS, rWS);
    if (aw == 1) {
      dma1(sb + L_GW, mkrsrc(gw, 1024), 0, lane * 16 + BIAS);
      touch(sb + L_DUMMY, (const char*)code + min(((idx & 7) * 64 + lane) * 128, code_len));
    } else {
      const int ch = 64 * j2 + lane;
      const float lr = bf2f(lre[ch]), li = bf2f(lim[ch]);
      const float dt = unpk(pk(sp(expf(bf2f(ldt[ch])))))[0];
      float e = expf(dt * lr), s, c;
      sincos_cw(dt * li, s, c);
      const cf lam = {e * c, e * s};
      const float inv = 2.0f / (lr * lr + li * li), nr = lam.re - 1.0f;
      const cf l2 = cmul(lam, lam), l4 = cmul(l2, l2), l8 = cmul(l4, l4), l16 = cmul(l8, l8), l32 = cmul(l16, l16), l64 = cmul(l32, l32);
      cf t = cmul(l64, l64);
      t = cmul(t, t);
      t = cmul(t, t);
      t = cmul(t, t);
      *(LAS f32x4*)(size_t)(sb + L_POW + lane * 16) = f32x4{lam.re, l2.re, lam.im, l2.im};
      *(LAS f32x4*)(size_t)(sb + L_POW + 1024 + lane * 16) = f32x4{l8.re, l8.im, l16.re, l16.im};
      *(LAS f32x4*)(size_t)(sb + L_POW + 2048 + lane * 16) = f32x4{(nr * lr + lam.im * li) * inv, (lam.im * lr - nr * li) * inv, l32.re, l32.im};
      *(LAS f32x4*)(size_t)(sb + L_POW + 3072 + lane * 16) = f32x4{l64.re, l64.im, t.re, t.im};
    }
    WAITV1(0, ep);
    const u64 tag = next_tag(ep[0], ep[1]);
    const u32 tlo = (u32)tag, thi = (u32)(tag >> 32);
    {
      f32x4 gacc[2] = {};
      u32 ax = sb + (32 * aw + m) * 256;
      const u32 ag = sb + L_GW + g * 16;
#pragma nounroll
      for (int s = 0; s < 4; s++) {
        LBAR();
#pragma unroll
        for (int ks = 0; ks < 4; ks++) {
          const bf16x8 gv = lds16(ag + (s ^ kx) * 256 + ks * 64);
#pragma unroll
          for (int u = 0; u < 2; u++) gacc[u] = __builtin_amdgcn_mfma_f32_16x16x32_bf16(lds16(ax + u * 4096 + (((4 * ks + g) ^ m) << 4)), gv, gacc[u], 0, 0, 0);
        }
        ax += s == 2 ? -2 * SLOT : SLOT;
      }
      LBAR();
#pragma unroll
      for (int u = 0; u < 2; u++) {
        f32x4 gt;
#pragma unroll
        for (int r = 0; r < 4; r++) gt[r] = __builtin_amdgcn_rcpf(1.0f + __expf(-gacc[u][r]));
        if (m == 0) *(LAS f32x4*)(size_t)(sb + L_GATE + (32 * aw + 16 * u + 4 * g) * 4) = gt;
      }
    }
    {
      const u32 vs = BIAS + WS_STAT + ((i2 * 16 + 8 * hf) * 64 + 32 * aw + r32) * 16;
      u32x4 p[8];
      auto fetch = [&]() {
#pragma unroll
        for (int i = 0; i < 8; i++) p[i] = ld16_issue(vs + i * 1024, rWS);
        WAITV8(p);
        u32 d = 0;
#pragma unroll
        for (int i = 0; i < 8; i++) d |= tagdiff(p[i], tlo, thi);
        return none(d);
      };
      LBAR();
      LBAR();
      if (!fetch()) {
        const u64 t0 = __builtin_amdgcn_s_memrealtime();
        do { STAMP(44 + 10 * aw); } while (!fetch() && __builtin_amdgcn_s_memrealtime() - t0 < SPIN_TICKS);
      }
      auto sum8 = [&](int c) {
        return ((__uint_as_float(p[0][c]) + __uint_as_float(p[1][c])) + (__uint_as_float(p[2][c]) + __uint_as_float(p[3][c]))) +
               ((__uint_as_float(p[4][c]) + __uint_as_float(p[5][c])) + (__uint_as_float(p[6][c]) + __uint_as_float(p[7][c])));
      };
      const float s1 = sum8(0), s2 = sum8(2);
      const float mean = halves(s1) * (1.0f / FH);
      const float var = fmaxf(halves(s2) * (1.0f / FH) - mean * mean, 0.f);
      const float rstd = __builtin_amdgcn_rsqf(var + 1e-5f);
      *(LAS float*)(size_t)(sb + L_STAT + hf * 256 + (32 * aw + r32) * 4) = hf ? -mean * rstd : rstd;
      STAMP(45 + 10 * aw);
    }
    LBAR();
    LBAR();
    LBAR();
    const u32 vf1 = BIAS + WS_F1 + j2 * 1024 + lane * 16;
    if (aw == 0) {
      const bool near = g == (kq >> 2);
      spin_flags(near ? vf1 : OOB, rWS, tlo, thi, near);
      STAMP(46);
    }
    LBAR();
    LBAR();
    LBAR();
    {
      u32x4 w[2][8];
      int c0 = i2 - 32 + 16 * hf;
      const u32 ve = BIAS + WS_SEG + (j2 * 32 + c0) * 1024 + (32 * aw + r32) * 16;
      auto issue = [&]() {
        PIN(c0);
#pragma unroll
        for (int i = 0; i < 16; i++) w[i >> 3][i & 7] = ld16_issue((ve + i * 1024) | ((c0 + i) & OOB), rWS);
      };
      auto fresh = [&]() {
        WAITV8(w[0]);
        WAITV8(w[1]);
        PIN(c0);
        u32 d = 0;
#pragma unroll
        for (int i = 0; i < 16; i++) d |= tagdiff(w[i >> 3][i & 7], tlo, thi) & ~((c0 + i) >> 31);
        return none(d);
      };
      u32x4 f = ld16_issue(aw == 0 ? vf1 : OOB, rWS);
      issue();
      LBAR();
      WAITV1(16, f);
      if (aw == 0) {
        if (!flags_ok(f, tlo, thi, true)) { STAMP(51); spin_flags(vf1, rWS, tlo, thi, true); }
        STAMP(49);
        if (blockIdx.x == 0 && lane == 0) __hip_atomic_store((GAS u64*)(ws + WS_EPOCH), tag, __ATOMIC_RELAXED, __HIP_MEMORY_SCOPE_AGENT);
      }
      LBAR();
      LBAR();
      LBAR();
      if (!fresh()) {
        const u64 t0 = __builtin_amdgcn_s_memrealtime();
        do { STAMP(47 + 10 * aw); issue(); } while (!fresh() && __builtin_amdgcn_s_memrealtime() - t0 < SPIN_TICKS);
      }
      STAMP(48 + 10 * aw);
      const f32x4 pw = ldsf4(sb + L_POW + 3072 + (32 * aw + r32) * 16);
      const cf l64 = {pw[0], pw[1]};
      cf h = {__uint_as_float(w[0][0][0]), __uint_as_float(w[0][0][2])};
#pragma unroll
      for (int i = 1; i < 16; i++) h = cfma(l64, h, cf{__uint_as_float(w[i >> 3][i & 7][0]), __uint_as_float(w[i >> 3][i & 7][2])});
      auto sr = __builtin_amdgcn_permlane32_swap(__float_as_uint(h.re), __float_as_uint(h.re), false, false);
      auto si = __builtin_amdgcn_permlane32_swap(__float_as_uint(h.im), __float_as_uint(h.im), false, false);
      const cf c = cfma(cf{pw[2], pw[3]}, cf{__uint_as_float(sr[0]), __uint_as_float(si[0])}, cf{__uint_as_float(sr[1]), __uint_as_float(si[1])});
      if (hf == 0) *(LAS f32x2*)(size_t)(sb + L_CARRY + (32 * aw + r32) * 8) = f32x2{c.re, c.im};
      STAMP(50 + 10 * aw);
    }
#pragma nounroll
    for (int k = 0; k < 2; k++) {
      const int n = k ? 6 : 7;
      const u32 vf = aw == 1 ? BIAS + (k ? WS_F3 : WS_F2) + i2 * 256 + m * 16 : OOB;
#pragma nounroll
      for (int s = 0; s < n; s++) LBAR();
      LBAR();
      u32x4 f = ld16_issue(vf, rWS);
      LBAR();
      WAITV1(0, f);
      if (aw == 1 && !flags_ok(f, tlo, thi, true)) { STAMP(59); spin_flags(vf, rWS, tlo, thi, true); }
      if (aw == 1) STAMP(61 + k);
    }
#pragma nounroll
    for (int s = 34; s < 42; s++) LBAR();
    __builtin_amdgcn_endpgm();
  } else {
    MFMA_PRIO;
    const int cw = wave - 4, wr = cw >> 1, wc = cw & 1, qq = m >> 2, p = lane & 3;
    const __amdgpu_buffer_rsrc_t rS = __builtin_amdgcn_make_buffer_rsrc(ws, 0, WS_END, 0x00020000);
    const u64 ep = __hip_atomic_load((GAS u64*)(ws + WS_EPOCH), __ATOMIC_RELAXED, __HIP_MEMORY_SCOPE_AGENT);
    const int trow = 64 * i2 + 32 * wr + 8 * (m >> 2) + (m & 3);
    const float bias0 = bf2f(bsp[trow]), bias1 = bf2f(bsp[trow + 4]);
    const u64 tag = next_tag((u32)ep, (u32)(ep >> 32));
    const u32 tlo = (u32)tag, thi = (u32)(tag >> 32);
    const int tile0 = tb + 3 * wc;
    auto valid = [&](int n) { return tile0 + n >= 1 && !(narrow && wc == 1 && n >= 1); };
    const int rg = 2 * i2 + wr;

    u32 aR[4], aW[3];
#pragma unroll
    for (int ks = 0; ks < 4; ks++) aR[ks] = sb + m * 256 + (((4 * ks + g) ^ m) << 4);
#pragma unroll
    for (int n = 0; n < 3; n++) aW[n] = sb + 16384 + (8 * g + qq) * 192 + (((3 * wc + n) ^ (g & 1)) << 5) + p * 8;
    auto advR = [&](u32 d) {
#pragma unroll
      for (int ks = 0; ks < 4; ks++) { aR[ks] += d; PIN(aR[ks]); }
    };
    auto advW = [&](u32 d) {
#pragma unroll
      for (int n = 0; n < 3; n++) { aW[n] += d; PIN(aW[n]); }
    };
    auto step = [](int st) -> u32 { return st % 3 == 0 ? -2 * SLOT : SLOT; };

    bf16x8 xf[2][16];
    bf16x8 wf[2][3];
    f32x4 z2[2][3];
    advR(32 * wr * 256);
    __builtin_amdgcn_s_waitcnt(0xC07F);
    LBAR();
    xf[0][0] = lds16(aR[0]);
#pragma unroll
    for (int n = 0; n < 3; n++) wf[0][n] = ldtr2(aW[n], 768);
    xf[1][0] = lds16(aR[0] + 4096);
#pragma unroll
    for (int kk = 0; kk < 16; kk++) {
      const int k1 = kk + 1, ks1 = k1 & 3;
      const bool nx = kk < 15;
      bf16x8* cur = wf[kk & 1];
      bf16x8* nxt = wf[k1 & 1];
      if (nx && ks1 == 0) { LBAR(); advR(step(k1 >> 2)); advW(step(k1 >> 2)); }
      MFK(kk == 0, z2[0][0], xf[0][kk], cur[0]);
      if (nx) { xf[0][k1] = lds16(aR[ks1]); nxt[0] = ldtr2(aW[0] + ks1 * 6144, 768); }
      MFK(kk == 0, z2[0][1], xf[0][kk], cur[1]);
      if (nx) nxt[1] = ldtr2(aW[1] + ks1 * 6144, 768);
      MFK(kk == 0, z2[0][2], xf[0][kk], cur[2]);
      if (nx) nxt[2] = ldtr2(aW[2] + ks1 * 6144, 768);
      MFK(kk == 0, z2[1][0], xf[1][kk], cur[0]);
      if (nx) xf[1][k1] = lds16(aR[ks1] + 4096);
      MFK(kk == 0, z2[1][1], xf[1][kk], cur[1]);
      MFK(kk == 0, z2[1][2], xf[1][kk], cur[2]);
    }
    MFDONE6(z2[0][0], z2[0][1], z2[0][2], z2[1][0], z2[1][1], z2[1][2]);
    STAMP(0);
    u32 z2p[3][4];
    {
      if (tile0 == 0) {
        z2[0][0] = f32x4{0.f, 0.f, 0.f, 0.f};
        z2[1][0] = f32x4{0.f, 0.f, 0.f, 0.f};
        PIN(z2[0][0]);
      }
      f32x2 s1[4], s2[4];
#pragma unroll
      for (int n = 0; n < 3; n++)
#pragma unroll
        for (int k = 0; k < 4; k++) {
          const f32x2 v = gelu2(half(z2[k >> 1][n], k & 1));
          s1[k] = n ? s1[k] + v : v;
          s2[k] = n ? fma2(v, v, s2[k]) : v * v;
          z2p[n][k] = pk(v);
        }
      const float r1 = reduce8(s1, m), r2 = reduce8(s2, m);
      if (!(m & 1))
        __builtin_amdgcn_raw_buffer_store_b128(u32x4{__float_as_uint(r1), tlo, __float_as_uint(r2), thi}, rS,
                                               WS_STAT + ((i2 * 16 + 2 * j2 + wc) * 64 + 32 * wr + 8 * g + (m >> 1)) * 16, 0, 16);
    }
    STAMP(1);
    f32x4 rs0, rs1, sh0, sh1;
    auto norm = [&](int n) {
      const u32 as = sb + L_STAT + (32 * wr + 8 * g) * 4;
      if (n == 0) { rs0 = ldsf4(as); rs1 = ldsf4(as + 16); sh0 = ldsf4(as + 256); sh1 = ldsf4(as + 272); }
      u32x4 o;
#pragma unroll
      for (int k = 0; k < 4; k++) o[k] = pk(fma2(unpk(z2p[n][k]), half(k >> 1 ? rs1 : rs0, k & 1), half(k >> 1 ? sh1 : sh0, k & 1)));
      if (n == 0 && tile0 == 0) o = u32x4{0u, 0u, 0u, 0u};
      __builtin_amdgcn_raw_buffer_store_b128(o, rS, n == 0 || valid(n) ? WS_Z + (rg * 48 + tile0 + n) * 1024 + lane * 16 : OOB, 0, 16);
      if (n == 2) STAMP(5);
    };
    bool pub1 = false;
    const u32 f1 = WS_F1 + ((j2 * 32 + i2) * 4 + cw) * 8;
    f32x4 ur[2][2], ui[2][2];
    {
      bf16x8 bf[2][4];
      advR(SLOT + 32 * (wc - wr) * 256);
      LBAR();
#pragma unroll
      for (int c = 0; c < 4; c++) bf[0][c] = lds16(aR[0] + (c >> 1) * 16384 + (c & 1) * 4096);
#pragma unroll
      for (int kk = 0; kk < 16; kk++) {
        const int k1 = kk + 1, ks1 = k1 & 3;
        const bool nx = kk < 15;
        bf16x8* cur = bf[kk & 1];
        bf16x8* nxt = bf[k1 & 1];
        if (nx && ks1 == 0) { LBAR(); advR(step(4 + (k1 >> 2))); }
        if (kk == 11) { norm(0); norm(1); norm(2); }
        if (kk >= 13 && !pub1) { try_flag(pub1, false, ws, f1, tag, lane); if (pub1) STAMP(7); }
#pragma unroll
        for (int c = 0; c < 4; c++) {
          if (c < 2) MFK(kk == 0, ur[0][c], xf[0][kk], cur[c]); else MFK(kk == 0, ui[0][c - 2], xf[0][kk], cur[c]);
          if (nx) nxt[c] = lds16(aR[ks1] + (c >> 1) * 16384 + (c & 1) * 4096);
        }
#pragma unroll
        for (int c = 0; c < 4; c++) {
          if (c < 2) MFK(kk == 0, ur[1][c], xf[1][kk], cur[c]); else MFK(kk == 0, ui[1][c - 2], xf[1][kk], cur[c]);
        }
      }
    }
    MFDONE4(ur[1][0], ur[1][1], ui[1][0], ui[1][1]);
    STAMP(2);
    cf pre[2], mul[2], wend[2];
    const u32 ap = sb + L_POW + (32 * wc + m) * 16;
#pragma unroll
    for (int a = 0; a < 2; a++) {
      const f32x4 t0 = ldsf4(ap + a * 256), t1 = ldsf4(ap + a * 256 + 1024);
      f32x2 er = ur[0][a].xy, ei = ui[0][a].xy;
#pragma unroll
      for (int k = 1; k < 4; k++) {
        const f32x2 nr = fma2(sp(t0[1]), er, fma2(sp(-t0[3]), ei, half(ur[k >> 1][a], k & 1)));
        const f32x2 ni = fma2(sp(t0[1]), ei, fma2(sp(t0[3]), er, half(ui[k >> 1][a], k & 1)));
        er = nr; ei = ni;
      }
      const cf e = cfma(cf{t0[0], t0[2]}, cf{er[0], ei[0]}, cf{er[1], ei[1]});
      float xr[4], xi[4];
      rows4(e.re, xr);
      rows4(e.im, xi);
      const cf l8 = {t1[0], t1[1]}, l16 = {t1[2], t1[3]};
      const cf e0 = {xr[0], xi[0]}, e1 = cfma(l8, e0, cf{xr[1], xi[1]}), e2 = cfma(l8, e1, cf{xr[2], xi[2]});
      wend[a] = cfma(l8, e2, cf{xr[3], xi[3]});
      const cf lo = (g & 1) ? e0 : cf{0.f, 0.f}, hi = (g & 1) ? e2 : e1;
      pre[a] = (g & 2) ? hi : lo;
      const cf q1 = (g & 1) ? l8 : cf{1.f, 0.f}, q2 = (g & 2) ? l16 : cf{1.f, 0.f};
      mul[a] = cmul(q1, q2);
      PIN(pre[a].re); PIN(pre[a].im); PIN(mul[a].re); PIN(mul[a].im);
      if (!pub1) { try_flag(pub1, a == 1, ws, f1, tag, lane); if (pub1) STAMP(7); }
    }
    if (wr == 0 && g < 2)
      *(LAS f32x2*)(size_t)(sb + L_W0 + (32 * wc + lane) * 8) = g ? f32x2{wend[1].re, wend[1].im} : f32x2{wend[0].re, wend[0].im};
    STAMP(3);
    f32x4 z1[2][3];
    u32 z1p[2][3][2];
    {
      advW(2 * SLOT);
      LBAR();
      STAMP(4);
#pragma unroll
      for (int n = 0; n < 3; n++) wf[0][n] = ldtr2(aW[n], 768);
#pragma unroll
      for (int kk = 0; kk < 16; kk++) {
        const int k1 = kk + 1, ks1 = k1 & 3;
        const bool nx = kk < 15;
        bf16x8* cur = wf[kk & 1];
        bf16x8* nxt = wf[k1 & 1];
        if (nx && ks1 == 0) { LBAR(); advW(step(8 + (k1 >> 2))); }
        if (kk == 1 && wr == 1) {
          cf e[2];
#pragma unroll
          for (int a = 0; a < 2; a++) {
            const f32x2 w0 = ldsf2(sb + L_W0 + (32 * wc + 16 * a + m) * 8);
            const f32x4 t2 = ldsf4(ap + a * 256 + 2048);
            const cf l32 = {t2[2], t2[3]};
            e[a] = cfma(l32, cf{w0[0], w0[1]}, wend[a]);
            pre[a] = cfma(mul[a], cf{w0[0], w0[1]}, pre[a]);
            mul[a] = cmul(mul[a], l32);
          }
          if (g < 2)
            __builtin_amdgcn_raw_buffer_store_b128(u32x4{__float_as_uint(g ? e[1].re : e[0].re), tlo, __float_as_uint(g ? e[1].im : e[0].im), thi}, rS,
                                                   WS_SEG + (j2 * 32 + i2) * 1024 + (32 * wc + lane) * 16, 0, 16);
        }
#pragma unroll
        for (int n = 0; n < 3; n++) {
          MFK(kk == 0, z1[0][n], cur[n], xf[0][kk]);
          if (nx) nxt[n] = ldtr2(aW[n] + ks1 * 6144, 768);
        }
#pragma unroll
        for (int n = 0; n < 3; n++) MFK(kk == 0, z1[1][n], cur[n], xf[1][kk]);
      }
    }
    STAMP(6);
    f32x4 ga[2][3] = {};
    {
      u32 aZ = sb + 16384 + 3 * wc * 1024 + lane * 16;
      bf16x8 fa[2][3], fb[2][2];
      advR(-SLOT + 32 * (wr - wc) * 256);
      asm volatile("" : "+v"(ga[0][0]), "+v"(ga[0][1]), "+v"(ga[0][2]), "+v"(ga[1][0]), "+v"(ga[1][1]), "+v"(ga[1][2]));
      LBAR();
      STAMP(8);
      fb[0][0] = lds16(aR[0]);
#pragma unroll
      for (int n = 0; n < 3; n++) fa[0][n] = lds16(aZ + n * 1024);
      fb[0][1] = lds16(aR[0] + 4096);
      auto group = [&](int ks, bool nx) {
        const int c = ks & 1, ks1 = (ks + 1) & 3;
        MF(ga[0][0], fa[c][0], fb[c][0]);
        if (nx) { fb[c ^ 1][0] = lds16(aR[ks1]); fa[c ^ 1][0] = lds16(aZ + ks1 * 6144); }
        MF(ga[0][1], fa[c][1], fb[c][0]);
        if (nx) fa[c ^ 1][1] = lds16(aZ + ks1 * 6144 + 1024);
        MF(ga[0][2], fa[c][2], fb[c][0]);
        if (nx) fa[c ^ 1][2] = lds16(aZ + ks1 * 6144 + 2048);
        MF(ga[1][0], fa[c][0], fb[c][1]);
        if (nx) fb[c ^ 1][1] = lds16(aR[ks1] + 4096);
        MF(ga[1][1], fa[c][1], fb[c][1]);
        MF(ga[1][2], fa[c][2], fb[c][1]);
      };
      bool pub = false;
      u32x4 hout[2];
#pragma unroll
      for (int s = 0; s < 16; s++) {
        group(0, true);
        group(1, true);
        group(2, true);
#pragma unroll
        for (int n = 0; n < 3; n++)
          if (s == n) {
#pragma unroll
            for (int u = 0; u < 2; u++) PIN(z1[u][n]);
#pragma unroll
            for (int k = 0; k < 4; k++) { z1p[k >> 1][n][k & 1] = pk(gelu2(half(z1[k >> 1][n], k & 1))); PIN(z1p[k >> 1][n][k & 1]); }
          }
        if (s >= 9 && s <= 10) try_flag(pub, s == 10, ws, WS_F2 + ((i2 * 8 + j2) * 4 + cw) * 8, tag, lane);
#pragma unroll
        for (int a = 0; a < 2; a++) {
          if (s == 5 + 2 * a) {
            PIN(ur[0][a]); PIN(ur[1][a]); PIN(ui[0][a]); PIN(ui[1][a]);
            const f32x2 cin = ldsf2(sb + L_CARRY + (32 * wc + 16 * a + m) * 8);
            const f32x4 t0 = ldsf4(ap + a * 256), t2 = ldsf4(ap + a * 256 + 2048);
            cf h = cfma(mul[a], cf{cin[0], cin[1]}, pre[a]);
            u32 o[2][4];
#pragma unroll
            for (int k = 0; k < 4; k++) {
              f32x2 pr = half(ur[k >> 1][a], k & 1), pi = half(ui[k >> 1][a], k & 1);
              const cf w = cfma(cf{t0[0], t0[2]}, cf{pr[0], pi[0]}, cf{pr[1], pi[1]});
              pr[1] = w.re; pi[1] = w.im;
              const f32x2 hr = fma2(t0.xy, sp(h.re), fma2(-t0.zw, sp(h.im), pr));
              const f32x2 hi = fma2(t0.xy, sp(h.im), fma2(t0.zw, sp(h.re), pi));
              h = {hr[1], hi[1]};
              o[0][k] = pk(fma2(sp(t2[0]), hr, sp(-t2[1]) * hi));
              o[1][k] = pk(fma2(sp(-t2[0]), hi, sp(-t2[1]) * hr));
            }
            const bool sw = m & 8;
#pragma unroll
            for (int c = 0; c < 2; c++) {
              hout[c] = sw ? u32x4{o[c][2], o[c][3], o[c][0], o[c][1]} : u32x4{o[c][0], o[c][1], o[c][2], o[c][3]};
              PIN(hout[c]);
            }
          }
          if (s == 6 + 2 * a) {
            const u32 off = WS_H + (rg * 32 + 4 * j2 + 2 * wc + a) * 1024 + (m * 4 + g) * 16;
#pragma unroll
            for (int c = 0; c < 2; c++) __builtin_amdgcn_raw_buffer_store_b128(hout[c], rS, off + c * (64 * 32 * 1024), 0, 16);
          }
        }
        if (s < 15) {
          LBAR();
          advR(step(s + 1));
          aZ += step(s + 1);
          PIN(aZ);
        }
        group(3, s < 15);
      }
    }
    MFDONE6(ga[0][0], ga[0][1], ga[0][2], ga[1][0], ga[1][1], ga[1][2]);
    STAMP(9);
#pragma unroll
    for (int n = 0; n < 3; n++) {
      u32 h[2][2];
#pragma unroll
      for (int u = 0; u < 2; u++)
#pragma unroll
        for (int k = 0; k < 2; k++) h[u][k] = pk(unpk(z1p[u][n][k]) * (half(ga[u][n], k) + (u ? bias1 : bias0)));
      u32x4 o = (g & 1) ? u32x4{h[1][0], h[1][1], h[0][0], h[0][1]} : u32x4{h[0][0], h[0][1], h[1][0], h[1][1]};
      if (tile0 + n == 44) o = u32x4{0u, 0u, 0u, 0u};
      __builtin_amdgcn_raw_buffer_store_b128(o, rS, valid(n) || tile0 + n == 44 ? WS_V + (rg * 48 + tile0 + n) * 1024 + lane * 16 : OOB, 0, 16);
    }
    STAMP(10);
    f32x4 ya[2][2] = {}, yb[2][2] = {};
    const int dcol = 64 * j2 + 32 * wc + 4 * g;
    u32x2 de[2];
#pragma unroll
    for (int n = 0; n < 2; n++) de[n] = *(const u32x2*)(dsk + dcol + 16 * n);
    {
      u32 aH[2];
#pragma unroll
      for (int u = 0; u < 2; u++) aH[u] = sb + SLOT + (wr * 8 + (g >> 1)) * 1024 + (8 * (g & 1) + qq) * 64 + p * 16 + ((u ^ (g & 1)) << 3);
      bf16x8 fa[2][2], fb[2][2];
      advR(SLOT + 16384 + 32 * (wc - wr) * 256);
      asm volatile("" : "+v"(ya[0][0]), "+v"(ya[0][1]), "+v"(ya[1][0]), "+v"(ya[1][1]));
      LBAR();
      fb[0][0] = ldtr2(aH[0], 256);
      fa[0][0] = lds16(aR[0]);
      fa[0][1] = lds16(aR[0] + 4096);
      fb[0][1] = ldtr2(aH[1], 256);
      auto group = [&](int ks, bool nx) {
        const int c = ks & 1, ks1 = (ks + 1) & 3;
        MF(ya[0][0], fa[c][0], fb[c][0]);
        if (nx) { fb[c ^ 1][0] = ldtr2(aH[0] + ks1 * 2048, 256); fa[c ^ 1][0] = lds16(aR[ks1]); }
        MF(ya[0][1], fa[c][1], fb[c][0]);
        if (nx) { fa[c ^ 1][1] = lds16(aR[ks1] + 4096); fb[c ^ 1][1] = ldtr2(aH[1] + ks1 * 2048, 256); }
        MF(ya[1][0], fa[c][0], fb[c][1]);
        MF(ya[1][1], fa[c][1], fb[c][1]);
      };
      int slot = 1;
      bool pub = false;
#pragma nounroll
      for (int s = 0; s < 7; s++) {
        group(0, true);
        group(1, true);
        group(2, true);
        if (!pub) { try_flag(pub, s == 2, ws, WS_F3 + ((i2 * 8 + j2) * 4 + cw) * 8, tag, lane); if (pub) STAMP(15); }
        LBAR();
        const u32 d = slot == 2 ? -2 * SLOT : SLOT;
        slot = slot == 2 ? 0 : slot + 1;
        advR(d);
        aH[0] += d; aH[1] += d;
        group(3, true);
      }
      group(0, true);
      group(1, true);
      group(2, true);
      group(3, false);
    }
    MFDONE4(ya[0][0], ya[0][1], ya[1][0], ya[1][1]);
    STAMP(11);
    float omg[2];
#pragma unroll
    for (int u = 0; u < 2; u++) {
      const float gt = *(const LAS float*)(size_t)(sb + L_GATE + (32 * wr + 16 * u + m) * 4);
      omg[u] = 1.0f - gt;
#pragma unroll
      for (int n = 0; n < 2; n++) {
        const u32x2 xe = *(const LAS u32x2*)(size_t)(sb + 32768 + (32 * wr + 16 * u + m) * 128 + (((4 * wc + 2 * n + (g >> 1)) ^ (m >> 1)) << 4) + (g & 1) * 8);
        const f32x2 t0 = sp(gt) * fma2(unpk(xe[0]), unpk(de[n][0]), ya[u][n].xy), t1 = sp(gt) * fma2(unpk(xe[1]), unpk(de[n][1]), ya[u][n].zw);
        ya[u][n] = f32x4{t0[0], t0[1], t1[0], t1[1]};
        PIN(ya[u][n]);
      }
      PIN(omg[u]);
    }
    {
      u32 aV[2], aO[2];
#pragma unroll
      for (int u = 0; u < 2; u++) {
        aV[u] = sb + wr * 8192 + lane * 16 + ((u ^ (g & 1)) << 3);
        aO[u] = sb + 16384 + (4 * g + qq) * 128 + (((2 * wc + u) ^ (2 * (g & 1) + (qq >> 1))) << 5) + p * 8;
      }
      bf16x8 fa[2][2], fb[2][2];
      auto rdV = [&](int u, int ks) { return cat(lds8(aV[u] + ks * 2048), lds8(aV[u] + ks * 2048 + 1024)); };
      asm volatile("" : "+v"(yb[0][0]), "+v"(yb[0][1]), "+v"(yb[1][0]), "+v"(yb[1][1]));
      LBAR();
      fb[0][0] = rdV(0, 0);
      fa[0][0] = ldtr2(aO[0], 2048);
      fa[0][1] = ldtr2(aO[1], 2048);
      fb[0][1] = rdV(1, 0);
      auto group = [&](int ks, bool nx) {
        const int c = ks & 1, ks1 = (ks + 1) & 3;
        MF(yb[0][0], fa[c][0], fb[c][0]);
        if (nx) { fb[c ^ 1][0] = rdV(0, ks1); fa[c ^ 1][0] = ldtr2(aO[0] + ks1 * 4096, 2048); }
        MF(yb[0][1], fa[c][1], fb[c][0]);
        if (nx) { fa[c ^ 1][1] = ldtr2(aO[1] + ks1 * 4096, 2048); fb[c ^ 1][1] = rdV(1, ks1); }
        MF(yb[1][0], fa[c][0], fb[c][1]);
        MF(yb[1][1], fa[c][1], fb[c][1]);
      };
      int slot = 0;
#pragma nounroll
      for (int s = 0; s < 5; s++) {
        group(0, true);
        group(1, true);
        group(2, true);
        LBAR();
        const u32 d = slot == 2 ? -2 * SLOT : SLOT;
        slot = slot == 2 ? 0 : slot + 1;
        aV[0] += d; aV[1] += d; aO[0] += d; aO[1] += d;
        group(3, true);
      }
      group(0, true);
      group(1, false);
    }
    MFDONE4(yb[0][0], yb[0][1], yb[1][0], yb[1][1]);
    STAMP(12);
#pragma unroll
    for (int u = 0; u < 2; u++) {
      f32x4 o[2];
#pragma unroll
      for (int n = 0; n < 2; n++) o[n] = __builtin_elementwise_fma(f32x4{omg[u], omg[u], omg[u], omg[u]}, yb[u][n], ya[u][n]);
      const bool up = m & 8;
      f32x4 a, b;
#pragma unroll
      for (int r = 0; r < 4; r++) {
        const float t = dpp<0x128>(up ? o[0][r] : o[1][r]);
        a[r] = up ? t : o[0][r];
        b[r] = up ? o[1][r] : t;
      }
      float* po = out + (64 * i2 + 32 * wr + 4 * u + 8 * ((m >> 2) & 1) + (m & 3)) * 512 + dcol + (up ? 16 : 0);
      *(f32x4*)po = a;
      *(f32x4*)(po + 16 * 512) = b;
    }
    asm volatile(".Lfused_end_c:");
    STAMP(13);
  }
}
}

extern "C" void kernel_launch(void* const* d_in, const int* in_sizes, int n_in, void* d_out, int out_size, void* d_ws, size_t ws_size,
                              hipStream_t stream) {
  (void)hipFuncSetAttribute((const void*)fused_kernel, hipFuncAttributeMaxDynamicSharedMemorySize, LDS_BYTES);
  fused_kernel<<<256, 512, LDS_BYTES, stream>>>((const u16*)d_in[0], (const u16*)d_in[1], (const u16*)d_in[2], (const u16*)d_in[3],
                                                (const u16*)d_in[4], (const u16*)d_in[5], (const u16*)d_in[6], (const u16*)d_in[7],
                                                (const u16*)d_in[8], (const u16*)d_in[10], (const u16*)d_in[11], (const u16*)d_in[12],
                                                (const u16*)d_in[13], (const u16*)d_in[14], (float*)d_out, (char*)d_ws);
}
```

```cpp
#define OUTAUX 18
#include <hip/hip_runtime.h>
#include <stdint.h>

namespace {
typedef unsigned long long u64;
typedef unsigned short u16;
typedef unsigned u32;
typedef short bf16x8 __attribute__((ext_vector_type(8)));
typedef short s16x4 __attribute__((ext_vector_type(4)));
typedef float f32x4 __attribute__((ext_vector_type(4)));
typedef float f32x2 __attribute__((ext_vector_type(2)));
typedef __bf16 bf2 __attribute__((ext_vector_type(2)));
typedef int i32x4 __attribute__((ext_vector_type(4)));
typedef u32 u32x4 __attribute__((ext_vector_type(4)));
typedef u32 u32x2 __attribute__((ext_vector_type(2)));
#define LAS __attribute__((address_space(3)))
#define GAS __attribute__((address_space(1)))
#define DI __device__ __forceinline__

constexpr int FH = 688;
constexpr u32 SA = 16384, SZ = 24576, RZ = 3 * SA;
constexpr u32 L_GW = RZ + 3 * SZ, L_STAT = L_GW + 1024, L_GATE = L_STAT + 512, L_CARRY = L_GATE + 256, L_W0 = L_CARRY + 512;
constexpr u32 L_POW = L_W0 + 512;
constexpr u32 L_DUMMY = L_POW + 4096;
constexpr u32 LX = L_DUMMY + 256;
constexpr u32 LDS_BYTES = LX + 32768;
constexpr u32 uslot(int s) { return s == 1 ? RZ + 40960 : s == 2 ? RZ : LX; }
constexpr u32 wslot(int s) { return s == 1 ? RZ + SZ : s == 2 ? LX : RZ + 2 * SZ; }
constexpr u32 ring(int s, u32 sz) { return (s % 3) * sz; }

constexpr u32 WS_EPOCH = 0;
constexpr u32 WS_F1 = 4096, WS_F2 = WS_F1 + 8192, WS_F3 = WS_F2 + 8192;
constexpr u32 WS_STAT = 65536;
constexpr u32 WS_SEG = WS_STAT + 32 * 16 * 64 * 16;
constexpr u32 WS_Z = 1u << 20;
constexpr u32 WS_H = WS_Z + 64 * 48 * 1024;
constexpr u32 WS_V = WS_H + 2 * 64 * 32 * 1024;
constexpr u32 WS_END = WS_V + 64 * 48 * 1024;
constexpr u32 WS_STAMP = 16u << 20;
constexpr u64 SPIN_TICKS = 2000000;

constexpr u32 BIAS = 4096;
constexpr u32 OOB = 0x80000000u;
DI i32x4 mkrsrc(const void* p, u32 bytes) {
  u64 a = (u64)p - BIAS;
  i32x4 r;
  r[0] = __builtin_amdgcn_readfirstlane((u32)a);
  r[1] = __builtin_amdgcn_readfirstlane((u32)(a >> 32) & 0xffff);
  r[2] = bytes + BIAS;
  r[3] = 0x00020000;
  return r;
}
#define M0SET "s_mov_b32 m0, %0\n\ts_nop 0\n\t"
#define BL(v, o, f) "buffer_load_dwordx4 %" #v ", %1, %2 offen offset:" #o f " lds\n\t"
DI void dma1(u32 lds, i32x4 rs, u32 soff, u32 v0) {
  asm volatile(M0SET BL(3, 0, "")::"s"(lds), "s"(rs), "s"(soff), "v"(v0) : "memory");
}
DI void dma2(u32 lds, i32x4 rs, u32 soff, const u32* v) {
  asm volatile(M0SET BL(3, 0, "") BL(4, 1024, "")::"s"(lds), "s"(rs), "s"(soff), "v"(v[0]), "v"(v[1]) : "memory");
}
DI void dma4(u32 lds, i32x4 rs, u32 soff, const u32* v) {
  asm volatile(M0SET BL(3, 0, "") BL(4, 1024, "") BL(5, 2048, "") BL(6, 3072, "")::"s"(lds), "s"(rs), "s"(soff), "v"(v[0]), "v"(v[1]), "v"(v[2]), "v"(v[3]) : "memory");
}
DI void dma2c(u32 lds, i32x4 rs, u32 soff, u32 v0) {
  asm volatile(M0SET BL(3, 0, " sc1") BL(3, 1024, " sc1")::"s"(lds), "s"(rs), "s"(soff), "v"(v0) : "memory");
}
DI void dma4c(u32 lds, i32x4 rs, u32 soff, u32 v0) {
  asm volatile(M0SET BL(3, 0, " sc1") BL(3, 1024, " sc1") BL(3, 2048, " sc1") BL(3, 3072, " sc1")::"s"(lds), "s"(rs), "s"(soff), "v"(v0) : "memory");
}
DI void touch(u32 lds, const void* p) {
  asm volatile("s_mov_b32 m0, %0\n\ts_nop 0\n\tglobal_load_lds_dword %1, off" ::"s"(lds), "v"(p) : "memory");
}
DI u32x4 ld16_issue(u32 voff, i32x4 rs) {
  u32x4 r;
  asm volatile("buffer_load_dwordx4 %0, %1, %2, 0 offen sc1" : "=v"(r) : "v"(voff), "s"(rs) : "memory");
  return r;
}
DI u32x2 ld8_issue(u32 voff, i32x4 rs) {
  u32x2 r;
  asm volatile("buffer_load_dwordx2 %0, %1, %2, 0 offen sc1" : "=v"(r) : "v"(voff), "s"(rs) : "memory");
  return r;
}
#define WAITV(n) asm volatile("s_waitcnt vmcnt(%0)" ::"n"(n) : "memory")
#define WAITV1(n, a) asm volatile("s_waitcnt vmcnt(%1)" : "+v"(a) : "n"(n) : "memory")
#define WAITV4(n, a, b, c, d) asm volatile("s_waitcnt vmcnt(%4)" : "+v"(a), "+v"(b), "+v"(c), "+v"(d) : "n"(n) : "memory")
#define WAITV8(a) asm volatile("s_waitcnt vmcnt(0)" : "+v"(a[0]), "+v"(a[1]), "+v"(a[2]), "+v"(a[3]), "+v"(a[4]), "+v"(a[5]), "+v"(a[6]), "+v"(a[7])::"memory")
#define LBAR() asm volatile("s_waitcnt lgkmcnt(0)\n\ts_barrier" ::: "memory")
#define PIN(a) asm volatile("" : "+v"(a))
#define MF(c, a, b) asm volatile("v_mfma_f32_16x16x32_bf16 %0, %1, %2, %0" : "+v"(c) : "v"(a), "v"(b))
#define MF0(c, a, b) asm volatile("v_mfma_f32_16x16x32_bf16 %0, %1, %2, 0" : "=&v"(c) : "v"(a), "v"(b))
#define MFK(first, c, a, b) do { if (first) MF0(c, a, b); else MF(c, a, b); } while (0)
#define MFDONE4(a, b, c, d) asm volatile("s_nop 7\n\ts_nop 1" : "+v"(a), "+v"(b), "+v"(c), "+v"(d))
#define MFDONE6(a, b, c, d, e, f) asm volatile("s_nop 7\n\ts_nop 1" : "+v"(a), "+v"(b), "+v"(c), "+v"(d), "+v"(e), "+v"(f))

DI bf16x8 lds16(u32 a) { return *(const LAS bf16x8*)(size_t)a; }
DI f32x4 ldsf4(u32 a) { return *(const LAS f32x4*)(size_t)a; }
DI f32x2 ldsf2(u32 a) { return *(const LAS f32x2*)(size_t)a; }
DI s16x4 lds8(u32 a) { return *(const LAS s16x4*)(size_t)a; }
DI s16x4 ldstr(u32 a) { return __builtin_amdgcn_ds_read_tr16_b64_v4i16((LAS s16x4*)(size_t)a); }
DI bf16x8 cat(s16x4 lo, s16x4 hi) { return __builtin_shufflevector(lo, hi, 0, 1, 2, 3, 4, 5, 6, 7); }
DI bf16x8 ldtr2(u32 a, u32 d) { return cat(ldstr(a), ldstr(a + d)); }
DI float bf2f(u16 v) { return __uint_as_float((u32)v << 16); }
DI u32 pk(f32x2 v) { return __builtin_bit_cast(u32, __builtin_convertvector(v, bf2)); }
DI f32x2 unpk(u32 v) { return f32x2{__uint_as_float(v << 16), __uint_as_float(v & 0xffff0000u)}; }
DI f32x2 fma2(f32x2 a, f32x2 b, f32x2 c) { return __builtin_elementwise_fma(a, b, c); }
DI f32x2 sp(float a) { return f32x2{a, a}; }
DI f32x2 half(f32x4 v, int h) { return h ? v.zw : v.xy; }
DI f32x2 gelu2(f32x2 x) {
  f32x2 a = x * fma2(sp(-0.1029432397f), x * x, sp(-2.302208198f));
  f32x2 e = f32x2{__builtin_amdgcn_exp2f(a[0]), __builtin_amdgcn_exp2f(a[1])} + 1.0f;
  return x * f32x2{__builtin_amdgcn_rcpf(e[0]), __builtin_amdgcn_rcpf(e[1])};
}
struct cf { float re, im; };
DI float add1(float a, float b) { float r; asm("v_add_f32 %0, %1, %2" : "=v"(r) : "v"(a), "v"(b)); return r; }
DI float mul1(float a, float b) { float r; asm("v_mul_f32 %0, %1, %2" : "=v"(r) : "v"(a), "v"(b)); return r; }
DI float fma1(float a, float b, float c) { float r; asm("v_fma_f32 %0, %1, %2, %3" : "=v"(r) : "v"(a), "v"(b), "v"(c)); return r; }
DI float fnma1(float a, float b, float c) { float r; asm("v_fma_f32 %0, -%1, %2, %3" : "=v"(r) : "v"(a), "v"(b), "v"(c)); return r; }
DI f32x2 fma2h(f32x2 a, f32x2 b, f32x2 c) { f32x2 r; asm("v_pk_fma_f32 %0, %1, %2, %3 op_sel:[1,0,0]" : "=v"(r) : "v"(a), "v"(b), "v"(c)); return r; }
DI f32x2 fnma2h(f32x2 a, f32x2 b, f32x2 c) {
  f32x2 r;
  asm("v_pk_fma_f32 %0, %1, %2, %3 op_sel:[1,0,0] neg_lo:[1,0,0] neg_hi:[1,0,0]" : "=v"(r) : "v"(a), "v"(b), "v"(c));
  return r;
}
DI cf cmul(cf a, cf b) { return {fnma1(a.im, b.im, mul1(a.re, b.re)), fma1(a.im, b.re, mul1(a.re, b.im))}; }
DI cf cfma(cf a, cf b, cf c) { return {fma1(a.re, b.re, fnma1(a.im, b.im, c.re)), fma1(a.re, b.im, fma1(a.im, b.re, c.im))}; }
DI void sincos_cw(float x, float& s, float& c) {
  float k = __builtin_rintf(x * 0.6366197724f);
  float r = __builtin_fmaf(k, -1.57079637050628662109375f, x);
  r = __builtin_fmaf(k, 4.37113900018624283e-8f, r);
  float r2 = r * r;
  float ps = __builtin_fmaf(r2, __builtin_fmaf(r2, __builtin_fmaf(r2, 2.7557314297e-06f, -1.9841270114e-04f), 8.3333337680e-03f), -1.6666667163e-01f);
  float sr = __builtin_fmaf(r * r2, ps, r);
  float pc = __builtin_fmaf(r2, __builtin_fmaf(r2, __builtin_fmaf(r2, -2.7557314297e-07f, 2.4801587642e-05f), -1.3888889225e-03f), 4.1666667908e-02f);
  float cr = __builtin_fmaf(r2 * r2, pc, __builtin_fmaf(r2, -0.5f, 1.0f));
  int q = (int)k;
  float ss = (q & 1) ? cr : sr, cc = (q & 1) ? sr : cr;
  s = (q & 2) ? -ss : ss;
  c = ((q + 1) & 2) ? -cc : cc;
}
template <int CTRL>
DI float dpp(float v) {
  return __int_as_float(__builtin_amdgcn_update_dpp(0, __float_as_int(v), CTRL, 0xf, 0xf, true));
}
DI float reduce8(const f32x2* v, int m) {
  float a[4], b[2];
  const bool h3 = m & 8, h2 = m & 4, h1 = m & 2;
#pragma unroll
  for (int k = 0; k < 4; k++) {
    const float lo = v[k >> 1][k & 1], hi = v[2 + (k >> 1)][k & 1];
    a[k] = (h3 ? hi : lo) + dpp<0x140>(h3 ? lo : hi);
  }
#pragma unroll
  for (int k = 0; k < 2; k++) b[k] = (h2 ? a[k + 2] : a[k]) + dpp<0x141>(h2 ? a[k] : a[k + 2]);
  float c = (h1 ? b[1] : b[0]) + dpp<0x4E>(h1 ? b[0] : b[1]);
  return c + dpp<0xB1>(c);
}
DI void rows4(float v, float* o) {
  auto s = __builtin_amdgcn_permlane16_swap(__float_as_uint(v), __float_as_uint(v), false, false);
  auto e = __builtin_amdgcn_permlane32_swap(s[0], s[0], false, false);
  auto d = __builtin_amdgcn_permlane32_swap(s[1], s[1], false, false);
  o[0] = __uint_as_float(e[0]); o[1] = __uint_as_float(d[0]); o[2] = __uint_as_float(e[1]); o[3] = __uint_as_float(d[1]);
}
DI float halves(float v) {
  auto s = __builtin_amdgcn_permlane32_swap(__float_as_uint(v), __float_as_uint(v), false, false);
  return __uint_as_float(s[0]) + __uint_as_float(s[1]);
}
DI bool none(u32 d) { PIN(d); return __builtin_amdgcn_ballot_w64(d != 0) == 0; }
DI u32 tagdiff(u32x4 v, u32 lo, u32 hi) { return (v[1] ^ lo) | (v[3] ^ hi); }
DI bool flags_ok(u32x4 v, u32 lo, u32 hi, bool need) { return none(need ? (v[0] ^ lo) | (v[1] ^ hi) | (v[2] ^ lo) | (v[3] ^ hi) : 0u); }
DI void spin_flags(u32 voff, i32x4 rs, u32 lo, u32 hi, bool need) {
  const u64 t0 = __builtin_amdgcn_s_memrealtime();
  for (;;) {
    u32x4 v = ld16_issue(voff, rs);
    WAITV1(0, v);
    if (flags_ok(v, lo, hi, need) || __builtin_amdgcn_s_memrealtime() - t0 > SPIN_TICKS) break;
    __builtin_amdgcn_s_sleep(2);
  }
}
DI void put_flag(char* ws, u32 off, u64 tag, int lane) {
  WAITV(0);
  if (lane == 0) __hip_atomic_store((GAS u64*)(ws + off), tag, __ATOMIC_RELAXED, __HIP_MEMORY_SCOPE_AGENT);
}
DI u64 next_tag(u32 lo, u32 hi) {
  return (((u64)(u32)__builtin_amdgcn_readfirstlane(hi) << 32) | (u32)__builtin_amdgcn_readfirstlane(lo)) * 6364136223846793005ull + 1442695040888963407ull;
}
#define STAT_BAR 7
#define SLP6 3
#define SLP7 7
#define P2S 7
#define KXG0 3
#define KXV 3
#define KQ ((xcd & 3) * 4)
#define PRIO_D 0
#define PRIO_A 3
#define PRIO_C 2
#define LOADER_PRIO __builtin_amdgcn_s_setprio(PRIO_D)
#define MFMA_PRIO __builtin_amdgcn_s_setprio(PRIO_C)
DI void try_flag(bool& done, bool force, char* ws, u32 off, u64 tag, int lane) {
  if (!done) {
    u32 st;
    asm volatile("s_getreg_b32 %0, hwreg(HW_REG_IB_STS)" : "=s"(st));
    if (force || (st & 0x00C0000Fu) == 0) { put_flag(ws, off, tag, lane); done = true; }
  }
}
#define STAMP(k)
#define LSTAMP(k) STAMP(k)

__global__ __launch_bounds__(512, 2) void fused_kernel(const u16* x, const u16* lre, const u16* lim, const u16* bre, const u16* bim,
                                                       const u16* cre, const u16* cim, const u16* ldt, const u16* dsk, const u16* win,
                                                       const u16* wsp, const u16* bsp, const u16* wout, const u16* gw, float* out, char* ws) {
  extern __shared__ __attribute__((aligned(16))) char smem[];
  const int tid = threadIdx.x, wave = __builtin_amdgcn_readfirstlane(tid >> 6), lane = tid & 63;
  const int xcd = blockIdx.x & 7, idx = blockIdx.x >> 3;
  const int i2 = (xcd & 3) * 8 + (idx & 7), j2 = (xcd >> 2) * 4 + (idx >> 3);
  const bool narrow = (j2 & 3) == 3;
  const int tb = 6 * j2 - 2 * (j2 >> 2);
  const u32 sb = (u32)(size_t)(LAS char*)smem;
  const int m = lane & 15, g = lane >> 4;

  u64 code;
  int code_len;
  asm volatile("s_getpc_b64 %0\n.Lfused_begin:\n\ts_mov_b32 %1, .Lfused_end_c-.Lfused_begin\n\ts_max_i32 %1, %1, .Lfused_end_l-.Lfused_begin" : "=s"(code), "=s"(code_len)::"scc");

  const int kx = (i2 + j2) & 3;
  const int kq = KQ;

  if (wave >= 4) {
    MFMA_PRIO;
    const int cw = wave - 4, wr = cw >> 1, wc = cw & 1, qq = m >> 2, p = lane & 3;
    const __amdgpu_buffer_rsrc_t rS = __builtin_amdgcn_make_buffer_rsrc(ws, 0, WS_END, 0x00020000);
    const u64 ep = __hip_atomic_load((GAS u64*)(ws + WS_EPOCH), __ATOMIC_RELAXED, __HIP_MEMORY_SCOPE_AGENT);
    const u64 tag = next_tag((u32)ep, (u32)(ep >> 32));
    const u32 tlo = (u32)tag, thi = (u32)(tag >> 32);
    const int tile0 = tb + 3 * wc;
    auto valid = [&](int n) { return tile0 + n >= 1 && !(narrow && wc == 1 && n >= 1); };
    const int rg = 2 * i2 + wr;

    u32 aR[4], aW[3];
#pragma unroll
    for (int ks = 0; ks < 4; ks++) aR[ks] = sb + m * 256 + (((4 * ks + g) ^ m) << 4);
#pragma unroll
    for (int n = 0; n < 3; n++) aW[n] = sb + RZ + (8 * g + qq) * 192 + (((3 * wc + n) ^ (g & 1)) << 5) + p * 8;
    auto advR = [&](u32 d) {
#pragma unroll
      for (int ks = 0; ks < 4; ks++) { aR[ks] += d; PIN(aR[ks]); }
    };
    auto advW = [&](u32 d) {
#pragma unroll
      for (int n = 0; n < 3; n++) { aW[n] += d; PIN(aW[n]); }
    };
    auto step = [](int st, u32 sz) -> u32 { return ring(st, sz) - ring(st - 1, sz); };

    bf16x8 xf[2][16];
    bf16x8 wf[2][3];
    f32x4 z2[2][3];
    advR(32 * wr * 256);
    __builtin_amdgcn_s_waitcnt(0xC07F);
    LBAR();
    xf[0][0] = lds16(aR[0]);
#pragma unroll
    for (int n = 0; n < 3; n++) wf[0][n] = ldtr2(aW[n], 768);
    xf[1][0] = lds16(aR[0] + 4096);
#pragma unroll
    for (int kk = 0; kk < 16; kk++) {
      const int k1 = kk + 1, ks1 = k1 & 3;
      const bool nx = kk < 15;
      bf16x8* cur = wf[kk & 1];
      bf16x8* nxt = wf[k1 & 1];
      if (nx && ks1 == 0) { LBAR(); advR(step(k1 >> 2, SA)); advW(step(k1 >> 2, SZ)); }
      MFK(kk == 0, z2[0][0], xf[0][kk], cur[0]);
      if (nx) { xf[0][k1] = lds16(aR[ks1]); nxt[0] = ldtr2(aW[0] + ks1 * 6144, 768); }
      MFK(kk == 0, z2[0][1], xf[0][kk], cur[1]);
      if (nx) nxt[1] = ldtr2(aW[1] + ks1 * 6144, 768);
      MFK(kk == 0, z2[0][2], xf[0][kk], cur[2]);
      if (nx) nxt[2] = ldtr2(aW[2] + ks1 * 6144, 768);
      MFK(kk == 0, z2[1][0], xf[1][kk], cur[0]);
      if (nx) xf[1][k1] = lds16(aR[ks1] + 4096);
      MFK(kk == 0, z2[1][1], xf[1][kk], cur[1]);
      MFK(kk == 0, z2[1][2], xf[1][kk], cur[2]);
    }
    MFDONE6(z2[0][0], z2[0][1], z2[0][2], z2[1][0], z2[1][1], z2[1][2]);
    STAMP(0);
    u32 z2p[3][4];
    {
      if (tile0 == 0) {
        z2[0][0] = f32x4{0.f, 0.f, 0.f, 0.f};
        z2[1][0] = f32x4{0.f, 0.f, 0.f, 0.f};
        PIN(z2[0][0]);
      }
      f32x2 s1[4], s2[4];
#pragma unroll
      for (int n = 0; n < 3; n++)
#pragma unroll
        for (int k = 0; k < 4; k++) {
          const f32x2 v = gelu2(half(z2[k >> 1][n], k & 1));
          s1[k] = n ? s1[k] + v : v;
          s2[k] = n ? fma2(v, v, s2[k]) : v * v;
          z2p[n][k] = pk(v);
        }
      const float r1 = reduce8(s1, m), r2 = reduce8(s2, m);
      if (!(m & 1))
        __builtin_amdgcn_raw_buffer_store_b128(u32x4{__float_as_uint(r1), tlo, __float_as_uint(r2), thi}, rS,
                                               WS_STAT + ((i2 * 16 + 2 * j2 + wc) * 64 + 32 * wr + 8 * g + (m >> 1)) * 16, 0, 16);
    }
    STAMP(1);
    f32x4 rs0, rs1, sh0, sh1;
    auto norm = [&](int n) {
      const u32 as = sb + L_STAT + (32 * wr + 8 * g) * 4;
      if (n == 0) { rs0 = ldsf4(as); rs1 = ldsf4(as + 16); sh0 = ldsf4(as + 256); sh1 = ldsf4(as + 272); }
      u32x4 o;
#pragma unroll
      for (int k = 0; k < 4; k++) o[k] = pk(fma2(unpk(z2p[n][k]), half(k >> 1 ? rs1 : rs0, k & 1), half(k >> 1 ? sh1 : sh0, k & 1)));
      if (n == 0 && tile0 == 0) o = u32x4{0u, 0u, 0u, 0u};
      __builtin_amdgcn_raw_buffer_store_b128(o, rS, n == 0 || valid(n) ? WS_Z + (rg * 48 + tile0 + n) * 1024 + lane * 16 : OOB, 0, 16);
      if (n == 2) STAMP(5);
    };
    bool pub1 = false;
    float gt[2];
    const u32 f1 = WS_F1 + ((j2 * 32 + i2) * 4 + cw) * 8;
    f32x4 ur[2][2], ui[2][2];
    {
      bf16x8 bf[2][4];
      advR(uslot(0) + 32 * (wc - wr) * 256);
      LBAR();
#pragma unroll
      for (int c = 0; c < 4; c++) bf[0][c] = lds16(aR[0] + (c >> 1) * 16384 + (c & 1) * 4096);
#pragma unroll
      for (int kk = 0; kk < 16; kk++) {
        const int k1 = kk + 1, ks1 = k1 & 3;
        const bool nx = kk < 15;
        bf16x8* cur = bf[kk & 1];
        bf16x8* nxt = bf[k1 & 1];
        if (kk == 11) {
          f32x4 ga[2];
          bf16x8 gf[2];
          u32 aG = sb + L_GW + g * 16 + kx * 256;
          PIN(aG);
          gf[0] = lds16(aG);
#pragma unroll
          for (int q = 0; q < 16; q++) {
            if ((q & 3) == 3 && q < 15) { aG ^= (((q + 1) >> 2) ^ (q >> 2)) * 256; PIN(aG); }
            if (q < 15) gf[(q + 1) & 1] = lds16(aG + ((q + 1) & 3) * 64);
            MFK(q == 0, ga[0], gf[q & 1], xf[0][q]);
            MFK(q == 0, ga[1], gf[q & 1], xf[1][q]);
          }
          asm volatile("s_nop 7\n\ts_nop 1" : "+v"(ga[0]), "+v"(ga[1]));
#pragma unroll
          for (int u = 0; u < 2; u++) { gt[u] = __builtin_amdgcn_rcpf(1.0f + __expf(-ga[u][0])); PIN(gt[u]); }
        }
        if (nx && ks1 == 0) { LBAR(); advR(uslot(k1 >> 2) - uslot((k1 >> 2) - 1)); }
        if (kk == 11) { norm(0); norm(1); norm(2); }
        if (kk >= 13 && !pub1) { try_flag(pub1, false, ws, f1, tag, lane); if (pub1) STAMP(7); }
#pragma unroll
        for (int c = 0; c < 4; c++) {
          if (c < 2) MFK(kk == 0, ur[0][c], xf[0][kk], cur[c]); else MFK(kk == 0, ui[0][c - 2], xf[0][kk], cur[c]);
          if (nx) nxt[c] = lds16(aR[ks1] + (c >> 1) * 16384 + (c & 1) * 4096);
        }
#pragma unroll
        for (int c = 0; c < 4; c++) {
          if (c < 2) MFK(kk == 0, ur[1][c], xf[1][kk], cur[c]); else MFK(kk == 0, ui[1][c - 2], xf[1][kk], cur[c]);
        }
      }
    }
    MFDONE4(ur[1][0], ur[1][1], ui[1][0], ui[1][1]);
    STAMP(2);
    cf pre[2], mul[2], wend[2];
    const u32 ap = sb + L_POW + (32 * wc + m) * 16;
    {
      f32x4 t0[2], t1[2];
      f32x2 er[2], ei[2];
      cf e[2];
#pragma unroll
      for (int a = 0; a < 2; a++) { t0[a] = ldsf4(ap + a * 256); t1[a] = ldsf4(ap + a * 256 + 1024); er[a] = ur[0][a].xy; ei[a] = ui[0][a].xy; }
      if (!pub1) { try_flag(pub1, false, ws, f1, tag, lane); if (pub1) STAMP(7); }
#pragma unroll
      for (int k = 1; k < 4; k++)
#pragma unroll
        for (int a = 0; a < 2; a++) {
          const f32x2 nr = fma2h(t0[a].xy, er[a], fnma2h(t0[a].zw, ei[a], half(ur[k >> 1][a], k & 1)));
          const f32x2 ni = fma2h(t0[a].xy, ei[a], fma2h(t0[a].zw, er[a], half(ui[k >> 1][a], k & 1)));
          er[a] = nr; ei[a] = ni;
        }
      float xr[2][4], xi[2][4];
#pragma unroll
      for (int a = 0; a < 2; a++) {
        e[a] = cfma(cf{t0[a][0], t0[a][2]}, cf{er[a][0], ei[a][0]}, cf{er[a][1], ei[a][1]});
        rows4(e[a].re, xr[a]);
        rows4(e[a].im, xi[a]);
      }
#pragma unroll
      for (int a = 0; a < 2; a++) {
        const cf l8 = {t1[a][0], t1[a][1]}, l16 = {t1[a][2], t1[a][3]};
        const cf e0 = {xr[a][0], xi[a][0]}, e1 = cfma(l8, e0, cf{xr[a][1], xi[a][1]}), e2 = cfma(l8, e1, cf{xr[a][2], xi[a][2]});
        wend[a] = cfma(l8, e2, cf{xr[a][3], xi[a][3]});
        const cf lo = (g & 1) ? e0 : cf{0.f, 0.f}, hi = (g & 1) ? e2 : e1;
        pre[a] = (g & 2) ? hi : lo;
        const cf q1 = (g & 1) ? l8 : cf{1.f, 0.f}, q2 = (g & 2) ? l16 : cf{1.f, 0.f};
        mul[a] = cmul(q1, q2);
      }
      PIN(pre[0].re); PIN(pre[0].im); PIN(mul[0].re); PIN(mul[0].im); PIN(pre[1].re); PIN(pre[1].im); PIN(mul[1].re); PIN(mul[1].im);
      if (!pub1) { try_flag(pub1, true, ws, f1, tag, lane); STAMP(7); }
    }
    if (wr == 0 && g < 2)
      *(LAS f32x2*)(size_t)(sb + L_W0 + (32 * wc + lane) * 8) = g ? f32x2{wend[1].re, wend[1].im} : f32x2{wend[0].re, wend[0].im};
    STAMP(3);
    f32x4 z1[2][3];
    u32 z1p[2][3][2];
    {
      advW(wslot(0) - RZ);
      LBAR();
      STAMP(4);
#pragma unroll
      for (int n = 0; n < 3; n++) wf[0][n] = ldtr2(aW[n], 768);
#pragma unroll
      for (int kk = 0; kk < 16; kk++) {
        const int k1 = kk + 1, ks1 = k1 & 3;
        const bool nx = kk < 15;
        bf16x8* cur = wf[kk & 1];
        bf16x8* nxt = wf[k1 & 1];
        if (nx && ks1 == 0) { LBAR(); advW(wslot(k1 >> 2) - wslot((k1 >> 2) - 1)); }
        if (kk == 1 && wr == 1) {
          cf e[2];
#pragma unroll
          for (int a = 0; a < 2; a++) {
            const f32x2 w0 = ldsf2(sb + L_W0 + (32 * wc + 16 * a + m) * 8);
            const f32x4 t2 = ldsf4(ap + a * 256 + 2048);
            const cf l32 = {t2[2], t2[3]};
            e[a] = cfma(l32, cf{w0[0], w0[1]}, wend[a]);
            pre[a] = cfma(mul[a], cf{w0[0], w0[1]}, pre[a]);
            mul[a] = cmul(mul[a], l32);
          }
          if (g < 2)
            __builtin_amdgcn_raw_buffer_store_b128(u32x4{__float_as_uint(g ? e[1].re : e[0].re), tlo, __float_as_uint(g ? e[1].im : e[0].im), thi}, rS,
                                                   WS_SEG + (j2 * 32 + i2) * 1024 + (32 * wc + lane) * 16, 0, 16);
        }
#pragma unroll
        for (int n = 0; n < 3; n++) {
          MFK(kk == 0, z1[0][n], cur[n], xf[0][kk]);
          if (nx) nxt[n] = ldtr2(aW[n] + ks1 * 6144, 768);
        }
#pragma unroll
        for (int n = 0; n < 3; n++) MFK(kk == 0, z1[1][n], cur[n], xf[1][kk]);
      }
    }
    STAMP(6);
    f32x4 ga[2][3] = {}, ya[2][2];
    const int dcol = 64 * j2 + 32 * wc + 4 * g;
    u32x2 de[2];
#pragma unroll
    for (int n = 0; n < 2; n++) de[n] = *(const u32x2*)(dsk + dcol + 16 * n);
    const int trow = 64 * i2 + 32 * wr + 8 * (m >> 2) + (m & 3);
    const float bias0 = bf2f(bsp[trow]), bias1 = bf2f(bsp[trow + 4]);
    {
      u32 aZ = sb + RZ + 3 * wc * 1024 + lane * 16;
      bf16x8 fa[2][3], fb[2][2];
      advR(SA - uslot(3) + 32 * (wr - wc) * 256);
      asm volatile("" : "+v"(ga[0][0]), "+v"(ga[0][1]), "+v"(ga[0][2]), "+v"(ga[1][0]), "+v"(ga[1][1]), "+v"(ga[1][2]));
      LBAR();
      STAMP(8);
      fb[0][0] = lds16(aR[0]);
#pragma unroll
      for (int n = 0; n < 3; n++) fa[0][n] = lds16(aZ + n * 1024);
      fb[0][1] = lds16(aR[0] + 4096);
      auto group = [&](int ks, bool nx) {
        const int c = ks & 1, ks1 = (ks + 1) & 3;
        MF(ga[0][0], fa[c][0], fb[c][0]);
        if (nx) { fb[c ^ 1][0] = lds16(aR[ks1]); fa[c ^ 1][0] = lds16(aZ + ks1 * 6144); }
        MF(ga[0][1], fa[c][1], fb[c][0]);
        if (nx) fa[c ^ 1][1] = lds16(aZ + ks1 * 6144 + 1024);
        MF(ga[0][2], fa[c][2], fb[c][0]);
        if (nx) fa[c ^ 1][2] = lds16(aZ + ks1 * 6144 + 2048);
        MF(ga[1][0], fa[c][0], fb[c][1]);
        if (nx) fb[c ^ 1][1] = lds16(aR[ks1] + 4096);
        MF(ga[1][1], fa[c][1], fb[c][1]);
        MF(ga[1][2], fa[c][2], fb[c][1]);
      };
      bool pub = false;
      u32x4 hout[2];
#pragma unroll
      for (int s = 0; s < 16; s++) {
        group(0, true);
        group(1, true);
        group(2, true);
#pragma unroll
        for (int n = 0; n < 3; n++)
          if (s == (n == 0 ? 10 : 11)) {
#pragma unroll
            for (int u = 0; u < 2; u++) PIN(z1[u][n]);
#pragma unroll
            for (int k = 0; k < 4; k++) { z1p[k >> 1][n][k & 1] = pk(gelu2(half(z1[k >> 1][n], k & 1))); PIN(z1p[k >> 1][n][k & 1]); }
          }
        if (s >= P2S + 1 && s <= P2S + 2) try_flag(pub, s == P2S + 2, ws, WS_F2 + ((i2 * 8 + j2) * 4 + cw) * 8, tag, lane);
#pragma unroll
        for (int a = 0; a < 2; a++) {
          if (false) {
            const u32 off = WS_H + (rg * 32 + 4 * j2 + 2 * wc + a) * 1024 + (m * 4 + g) * 16;
#pragma unroll
            for (int c = 0; c < 2; c++) __builtin_amdgcn_raw_buffer_store_b128(hout[c], rS, off + c * (64 * 32 * 1024), 0, 16);
          }
        }
#pragma unroll
        for (int a = 0; a < 2; a++) {
          if (s == 3 + 4 * a) {
            PIN(ur[0][a]); PIN(ur[1][a]); PIN(ui[0][a]); PIN(ui[1][a]);
            const f32x2 cin = ldsf2(sb + L_CARRY + (32 * wc + 16 * a + m) * 8);
            const f32x4 t0 = ldsf4(ap + a * 256), t2 = ldsf4(ap + a * 256 + 2048);
            const cf h0 = cfma(mul[a], cf{cin[0], cin[1]}, pre[a]);
            f32x2 hr = sp(h0.re), hi = sp(h0.im);
            const f32x2 cr = sp(t2[0]), ci = sp(-t2[1]);
            u32 o[2][4];
#pragma unroll
            for (int k = 0; k < 4; k++) {
              f32x2 pr = half(ur[k >> 1][a], k & 1), pi = half(ui[k >> 1][a], k & 1);
              const cf w = cfma(cf{t0[0], t0[2]}, cf{pr[0], pi[0]}, cf{pr[1], pi[1]});
              pr[1] = w.re; pi[1] = w.im;
              const f32x2 nr = fma2h(hr, t0.xy, fnma2h(hi, t0.zw, pr));
              const f32x2 ni = fma2h(hi, t0.xy, fma2h(hr, t0.zw, pi));
              hr = nr; hi = ni;
              o[0][k] = pk(fma2(cr, hr, ci * hi));
              o[1][k] = pk(fma2(-cr, hi, ci * hr));
            }
            const bool sw = m & 8;
#pragma unroll
            for (int c = 0; c < 2; c++) {
              hout[c] = sw ? u32x4{o[c][2], o[c][3], o[c][0], o[c][1]} : u32x4{o[c][0], o[c][1], o[c][2], o[c][3]};
              __builtin_amdgcn_raw_buffer_store_b128(hout[c], rS, WS_H + (rg * 32 + 4 * j2 + 2 * wc + a) * 1024 + (m * 4 + g) * 16 + c * (64 * 32 * 1024), 0, 16);
            }
          }
        }
        if (s == 12) {
#pragma unroll
          for (int u = 0; u < 2; u++)
#pragma unroll
            for (int n = 0; n < 2; n++) {
              const u32x2 xe = *(const LAS u32x2*)(size_t)(sb + LX + (32 * wr + 16 * u + m) * 128 + (((4 * wc + 2 * n + (g >> 1)) ^ (m >> 1)) << 4) + (g & 1) * 8);
              const f32x2 t0 = unpk(xe[0]) * unpk(de[n][0]), t1 = unpk(xe[1]) * unpk(de[n][1]);
              ya[u][n] = f32x4{t0[0], t0[1], t1[0], t1[1]};
              PIN(ya[u][n]);
            }
        }
        if (s < 15) {
          LBAR();
          advR(step(s + 2, SA));
          aZ += step(s + 1, SZ);
          PIN(aZ);
        }
        group(3, s < 15);
      }
    }
    MFDONE6(ga[0][0], ga[0][1], ga[0][2], ga[1][0], ga[1][1], ga[1][2]);
    STAMP(9);
#pragma unroll
    for (int n = 0; n < 3; n++) {
      u32 h[2][2];
#pragma unroll
      for (int u = 0; u < 2; u++)
#pragma unroll
        for (int k = 0; k < 2; k++) h[u][k] = pk(unpk(z1p[u][n][k]) * (half(ga[u][n], k) + (u ? bias1 : bias0)));
      u32x4 o = (g & 1) ? u32x4{h[1][0], h[1][1], h[0][0], h[0][1]} : u32x4{h[0][0], h[0][1], h[1][0], h[1][1]};
      if (tile0 + n == 44) o = u32x4{0u, 0u, 0u, 0u};
      __builtin_amdgcn_raw_buffer_store_b128(o, rS, valid(n) || tile0 + n == 44 ? WS_V + (rg * 48 + tile0 + n) * 1024 + lane * 16 : OOB, 0, 16);
    }
    STAMP(10);
    f32x4 yb[2][2] = {};
    {
      u32 aH[2];
#pragma unroll
      for (int u = 0; u < 2; u++) aH[u] = sb + 2 * SA + (wr * 8 + (g >> 1)) * 1024 + (8 * (g & 1) + qq) * 64 + p * 16 + ((u ^ (g & 1)) << 3);
      bf16x8 fa[2][2], fb[2][2];
      advR(RZ + SZ - SA + 32 * (wc - wr) * 256);
      asm volatile("" : "+v"(ya[0][0]), "+v"(ya[0][1]), "+v"(ya[1][0]), "+v"(ya[1][1]));
      LBAR();
      fb[0][0] = ldtr2(aH[0], 256);
      fa[0][0] = lds16(aR[0]);
      fa[0][1] = lds16(aR[0] + 4096);
      fb[0][1] = ldtr2(aH[1], 256);
      auto group = [&](int ks, bool nx) {
        const int c = ks & 1, ks1 = (ks + 1) & 3;
        MF(ya[0][0], fa[c][0], fb[c][0]);
        if (nx) { fb[c ^ 1][0] = ldtr2(aH[0] + ks1 * 2048, 256); fa[c ^ 1][0] = lds16(aR[ks1]); }
        MF(ya[0][1], fa[c][1], fb[c][0]);
        if (nx) { fa[c ^ 1][1] = lds16(aR[ks1] + 4096); fb[c ^ 1][1] = ldtr2(aH[1] + ks1 * 2048, 256); }
        MF(ya[1][0], fa[c][0], fb[c][1]);
        MF(ya[1][1], fa[c][1], fb[c][1]);
      };
      int slot = 1;
      bool pub = false;
#pragma nounroll
      for (int s = 0; s < 7; s++) {
        group(0, true);
        group(1, true);
        group(2, true);
        if (!pub) { try_flag(pub, s == 2, ws, WS_F3 + ((i2 * 8 + j2) * 4 + cw) * 8, tag, lane); if (pub) STAMP(15); }
        LBAR();
        const u32 da = slot == 1 ? -2 * SA : SA, dz = slot == 2 ? -2 * SZ : SZ;
        slot = slot == 2 ? 0 : slot + 1;
        advR(dz);
        aH[0] += da; aH[1] += da;
        group(3, true);
      }
      group(0, true);
      group(1, true);
      group(2, true);
      group(3, false);
    }
    STAMP(11);
    {
      u32 aV[2], aO[2];
#pragma unroll
      for (int u = 0; u < 2; u++) {
        aV[u] = sb + SA + wr * 8192 + lane * 16 + ((u ^ (g & 1)) << 3);
        aO[u] = sb + RZ + (4 * g + qq) * 128 + (((2 * wc + u) ^ (2 * (g & 1) + (qq >> 1))) << 5) + p * 8;
      }
      bf16x8 fa[2][2], fb[2][2];
      auto rdV = [&](int u, int ks) { return cat(lds8(aV[u] + ks * 2048), lds8(aV[u] + ks * 2048 + 1024)); };
      asm volatile("" : "+v"(yb[0][0]), "+v"(yb[0][1]), "+v"(yb[1][0]), "+v"(yb[1][1]));
      LBAR();
      fb[0][0] = rdV(0, 0);
      fa[0][0] = ldtr2(aO[0], 2048);
      fa[0][1] = ldtr2(aO[1], 2048);
      fb[0][1] = rdV(1, 0);
      auto group = [&](int ks, bool nx) {
        const int c = ks & 1, ks1 = (ks + 1) & 3;
        MF(yb[0][0], fa[c][0], fb[c][0]);
        if (nx) { fb[c ^ 1][0] = rdV(0, ks1); fa[c ^ 1][0] = ldtr2(aO[0] + ks1 * 4096, 2048); }
        MF(yb[0][1], fa[c][1], fb[c][0]);
        if (nx) { fa[c ^ 1][1] = ldtr2(aO[1] + ks1 * 4096, 2048); fb[c ^ 1][1] = rdV(1, ks1); }
        MF(yb[1][0], fa[c][0], fb[c][1]);
        MF(yb[1][1], fa[c][1], fb[c][1]);
      };
      int slot = 0;
#pragma nounroll
      for (int s = 0; s < 5; s++) {
        group(0, true);
        group(1, true);
        group(2, true);
        LBAR();
        const u32 da = slot == 1 ? -2 * SA : SA, dz = slot == 2 ? -2 * SZ : SZ;
        slot = slot == 2 ? 0 : slot + 1;
        aV[0] += da; aV[1] += da; aO[0] += dz; aO[1] += dz;
        group(3, true);
      }
      group(0, true);
      group(1, false);
    }
    const __amdgpu_buffer_rsrc_t rOut = __builtin_amdgcn_make_buffer_rsrc(out, 0, 2048 * 512 * 4, 0x00020000);
    MFDONE4(yb[0][0], yb[0][1], yb[1][0], yb[1][1]);
    STAMP(12);
#pragma unroll
    for (int u = 0; u < 2; u++) {
      f32x4 o[2];
#pragma unroll
      for (int n = 0; n < 2; n++) o[n] = gt[u] * ya[u][n] + (1.0f - gt[u]) * yb[u][n];
      const bool up = m & 8;
      f32x4 a, b;
#pragma unroll
      for (int r = 0; r < 4; r++) {
        const float t = dpp<0x128>(up ? o[0][r] : o[1][r]);
        a[r] = up ? t : o[0][r];
        b[r] = up ? o[1][r] : t;
      }
      const u32 po = ((64 * i2 + 32 * wr + 4 * u + 8 * ((m >> 2) & 1) + (m & 3)) * 512 + dcol + (up ? 16 : 0)) * 4;
      __builtin_amdgcn_raw_buffer_store_b128(__builtin_bit_cast(u32x4, a), rOut, po, 0, OUTAUX);
      __builtin_amdgcn_raw_buffer_store_b128(__builtin_bit_cast(u32x4, b), rOut, po + 16 * 2048, 0, OUTAUX);
    }
    asm volatile(".Lfused_end_c:");
    STAMP(13);
    __builtin_amdgcn_endpgm();
  } else if (wave >= 2) {
    const int aw = wave - 2, r32 = lane & 31, hf = lane >> 5;
    __builtin_amdgcn_s_setprio(PRIO_A);
    const i32x4 rWS = mkrsrc(ws, WS_END);
    u32x2 ep = ld8_issue(WS_EPOCH + BIAS, rWS);
    if (aw == 1) {
      dma1(sb + L_GW, mkrsrc(gw, 1024), 0, lane * 16 + BIAS);
      touch(sb + L_DUMMY, (const char*)code + min(((idx & 7) * 64 + lane) * 128, code_len));
    } else {
      const int ch = 64 * j2 + lane;
      const float lr = bf2f(lre[ch]), li = bf2f(lim[ch]);
      const float dt = unpk(pk(sp(expf(bf2f(ldt[ch])))))[0];
      float e = expf(dt * lr), s, c;
      sincos_cw(dt * li, s, c);
      const cf lam = {e * c, e * s};
      const float inv = 2.0f / (lr * lr + li * li), nr = lam.re - 1.0f;
      const cf l2 = cmul(lam, lam), l4 = cmul(l2, l2), l8 = cmul(l4, l4), l16 = cmul(l8, l8), l32 = cmul(l16, l16), l64 = cmul(l32, l32);
      cf t = cmul(l64, l64);
      t = cmul(t, t);
      t = cmul(t, t);
      t = cmul(t, t);
      *(LAS f32x4*)(size_t)(sb + L_POW + lane * 16) = f32x4{lam.re, l2.re, lam.im, l2.im};
      *(LAS f32x4*)(size_t)(sb + L_POW + 1024 + lane * 16) = f32x4{l8.re, l8.im, l16.re, l16.im};
      *(LAS f32x4*)(size_t)(sb + L_POW + 2048 + lane * 16) = f32x4{(nr * lr + lam.im * li) * inv, (lam.im * lr - nr * li) * inv, l32.re, l32.im};
      *(LAS f32x4*)(size_t)(sb + L_POW + 3072 + lane * 16) = f32x4{l64.re, l64.im, t.re, t.im};
    }
    WAITV1(0, ep);
    const u64 tag = next_tag(ep[0], ep[1]);
    const u32 tlo = (u32)tag, thi = (u32)(tag >> 32);
#pragma nounroll
    for (int s = 0; s < 5; s++) LBAR();
    {
      const u32 vs = BIAS + WS_STAT + ((i2 * 16 + 8 * hf) * 64 + 32 * aw + r32) * 16;
      u32x4 p[8];
      auto fetch = [&]() {
#pragma unroll
        for (int i = 0; i < 8; i++) p[i] = ld16_issue(vs + i * 1024, rWS);
        WAITV8(p);
        u32 d = 0;
#pragma unroll
        for (int i = 0; i < 8; i++) d |= tagdiff(p[i], tlo, thi);
        return none(d);
      };
      LBAR();
      LBAR();
      if (!fetch()) {
        const u64 t0 = __builtin_amdgcn_s_memrealtime();
        do { STAMP(44 + 10 * aw); } while (!fetch() && __builtin_amdgcn_s_memrealtime() - t0 < SPIN_TICKS);
      }
      auto sum8 = [&](int c) {
        auto two = [&](int i) { return add1(__uint_as_float(p[i][c]), __uint_as_float(p[i + 1][c])); };
        return add1(add1(two(0), two(2)), add1(two(4), two(6)));
      };
      const float s1 = sum8(0), s2 = sum8(2);
      const float mean = halves(s1) * (1.0f / FH);
      const float var = fmaxf(halves(s2) * (1.0f / FH) - mean * mean, 0.f);
      const float rstd = __builtin_amdgcn_rsqf(var + 1e-5f);
      *(LAS float*)(size_t)(sb + L_STAT + hf * 256 + (32 * aw + r32) * 4) = hf ? -mean * rstd : rstd;
      STAMP(45 + 10 * aw);
    }
    LBAR();
    LBAR();
    LBAR();
    const u32 vf1 = BIAS + WS_F1 + j2 * 1024 + lane * 16;
    if (aw == 0) {
      const bool near = g == (kq >> 2);
      spin_flags(near ? vf1 : OOB, rWS, tlo, thi, near);
      STAMP(46);
    }
    __builtin_amdgcn_s_setprio(0);
    LBAR();
    LBAR();
    {
      u32x4 w[2][8];
      int c0 = i2 - 32 + 16 * hf;
      const u32 ve = BIAS + WS_SEG + (j2 * 32 + c0) * 1024 + (32 * aw + r32) * 16;
      auto issue = [&]() {
        PIN(c0);
#pragma unroll
        for (int i = 0; i < 16; i++) w[i >> 3][i & 7] = ld16_issue((ve + i * 1024) | ((c0 + i) & OOB), rWS);
      };
      auto fresh = [&]() {
        WAITV8(w[0]);
        WAITV8(w[1]);
        PIN(c0);
        u32 d = 0;
#pragma unroll
        for (int i = 0; i < 16; i++) d |= tagdiff(w[i >> 3][i & 7], tlo, thi) & ~((c0 + i) >> 31);
        return none(d);
      };
      issue();
      LBAR();
      u32x4 f = ld16_issue(aw == 0 ? vf1 : OOB, rWS);
      LBAR();
      WAITV1(0, f);
      if (aw == 0) {
        if (!flags_ok(f, tlo, thi, true)) { STAMP(51); spin_flags(vf1, rWS, tlo, thi, true); }
        STAMP(49);
        if (blockIdx.x == 0 && lane == 0) __hip_atomic_store((GAS u64*)(ws + WS_EPOCH), tag, __ATOMIC_RELAXED, __HIP_MEMORY_SCOPE_AGENT);
      }
      if (!fresh()) {
        const u64 t0 = __builtin_amdgcn_s_memrealtime();
        do { STAMP(47 + 10 * aw); issue(); } while (!fresh() && __builtin_amdgcn_s_memrealtime() - t0 < SPIN_TICKS);
      }
      STAMP(48 + 10 * aw);
      LBAR();
      const f32x4 pw = ldsf4(sb + L_POW + 3072 + (32 * aw + r32) * 16);
      const cf l64 = {pw[0], pw[1]};
      cf h = {__uint_as_float(w[0][0][0]), __uint_as_float(w[0][0][2])};
#pragma unroll
      for (int i = 1; i < 16; i++) h = cfma(l64, h, cf{__uint_as_float(w[i >> 3][i & 7][0]), __uint_as_float(w[i >> 3][i & 7][2])});
      auto sr = __builtin_amdgcn_permlane32_swap(__float_as_uint(h.re), __float_as_uint(h.re), false, false);
      auto si = __builtin_amdgcn_permlane32_swap(__float_as_uint(h.im), __float_as_uint(h.im), false, false);
      const cf c = cfma(cf{pw[2], pw[3]}, cf{__uint_as_float(sr[0]), __uint_as_float(si[0])}, cf{__uint_as_float(sr[1]), __uint_as_float(si[1])});
      if (hf == 0) *(LAS f32x2*)(size_t)(sb + L_CARRY + (32 * aw + r32) * 8) = f32x2{c.re, c.im};
      STAMP(50 + 10 * aw);
    }
#pragma nounroll
    for (int k = 0; k < 2; k++) {
      const int n = k ? 6 : 9;
      const u32 vf = aw == 1 ? BIAS + (k ? WS_F3 : WS_F2) + i2 * 256 + m * 16 : OOB;
#pragma nounroll
      for (int s = 0; s < n; s++) LBAR();
      LBAR();
      u32x4 f = ld16_issue(vf, rWS);
      LBAR();
      WAITV1(0, f);
      if (aw == 1 && !flags_ok(f, tlo, thi, true)) { STAMP(59); spin_flags(vf, rWS, tlo, thi, true); }
      if (aw == 1) STAMP(61 + k);
    }
#pragma nounroll
    for (int s = 34; s < 42; s++) LBAR();
    __builtin_amdgcn_endpgm();
  } else {
    const int dw = wave;
    LOADER_PRIO;
    const int rowA = 64 * i2 + 32 * dw + g;
    u32 vX[4], vW[6];
#pragma unroll
    for (int j = 0; j < 4; j++) { vX[j] = (rowA + 8 * j) * 1024 + ((m ^ (4 * j + g)) << 4) + BIAS - 1024 * j; PIN(vX[j]); }
#pragma unroll
    for (int n = 0; n < 6; n++) {
      const int c = n * 64 + lane, k = c / 12, cc = c - 12 * k;
      const int o = max((64 * dw + k) * 2752 + 32 * tb - 32 + ((cc ^ (((k >> 3) & 1) << 1)) << 4), 0);
      vW[n] = narrow && cc >= 8 ? OOB : o + BIAS - 1024 * (n & 3);
      PIN(vW[n]);
    }
    __builtin_amdgcn_sched_barrier(0);
    const i32x4 rX = mkrsrc(x, 2048 * 512 * 2), rW = mkrsrc(win, 512 * 1376 * 2), rWS = mkrsrc(ws, WS_END);
    u32 na = sb, nz = sb + RZ;
    auto rot = [&]() { na += SA; nz += SZ; if (na == sb + 3 * SA) na = sb; if (nz == sb + RZ + 3 * SZ) nz = sb + RZ; };
    auto rows = [&](u32 lds, i32x4 rs, u32 soff, u32 second, const u32* v) { dma4(lds, rs, soff, v); dma4(lds + 4096, rs, soff + second, v); };
    auto issueX = [&](int s) { rows(na + dw * 8192, rX, (s ^ kx) * 256, 4 * 1024, vX); };
    auto issueW = [&](u32 base, int s, u32 col) {
#pragma unroll
      for (int q = 0; q < 2; q++) {
        const u32 l = base + dw * 12288 + q * 6144, o = (s ^ kx) * (128 * 2752) + q * (32 * 2752) + col;
        dma4(l, rW, o, vW);
        dma2(l + 4096, rW, o, vW + 4);
      }
    };
#pragma nounroll
    for (int s = 0; s < 2; s++) { issueX(s); issueW(nz, s, 2 * FH); rot(); }
    LSTAMP(42);
    const i32x4 rB = mkrsrc(dw ? bim : bre, 512 * 512 * 2), rA = mkrsrc(wsp, 2048 * 2048 * 2);
    u32 vB[4], vA[4];
#pragma unroll
    for (int j = 0; j < 4; j++) {
      vB[j] = (64 * j2 + 4 * j + g) * 1024 + ((m ^ (4 * j + g)) << 4) + BIAS - 1024 * j;
      vA[j] = (rowA + 8 * j) * 4096 + ((m ^ (4 * j + g)) << 4) + BIAS - 1024 * j;
      PIN(vB[j]); PIN(vA[j]);
    }
    u32 vZ = BIAS + WS_Z + (2 * dw * 48 + tb) * 1024 + lane * 16, vZ2 = narrow ? OOB : vZ;
    PIN(vZ); PIN(vZ2);
    WAITV(20);
#pragma nounroll
    for (int s = 0; s < 2; s++) { LBAR(); LSTAMP(s); issueX(s + 2); issueW(nz, s + 2, 2 * FH); rot(); WAITV(20); }
    auto issueB = [&](int s) {
      const u32 l = sb + uslot(s) + dw * 16384;
      rows(l, rB, (s ^ kx) * 256, 16 * 1024, vB);
      rows(l + 8192, rB, (s ^ kx) * 256 + 32 * 1024, 16 * 1024, vB);
    };
    auto kst = [&](int s) { return ((s + kq) & 15) ^ (s < 4 ? kx & KXG0 : kx); };
    auto issueA = [&](u32 base, int s) { rows(base + dw * 8192, rA, kst(s) * 256, 4 * 4096, vA); };
    auto issueZ = [&](u32 base, int s) {
#pragma unroll
      for (int q = 0; q < 2; q++) {
        const u32 l = base + dw * 12288 + q * 6144, o = kst(s) * (4 * 48 * 1024) + q * (48 * 1024);
        dma4c(l, rWS, o, vZ);
        dma2c(l + 4096, rWS, o + 4096, vZ2);
      }
    };
    LBAR(); LSTAMP(2); issueB(0); WAITV(16);
    LBAR(); LSTAMP(3); issueB(1); issueA(sb + SA, 0); issueA(sb + 2 * SA, 1); WAITV(32);
    LBAR(); LSTAMP(4); issueB(2); WAITV(32);
    LBAR(); LSTAMP(5); issueB(3);
    const i32x4 rC0 = mkrsrc(cre, 512 * 512 * 2), rC1 = mkrsrc(cim, 512 * 512 * 2), rO = mkrsrc(wout, FH * 512 * 2);
    u32 vC[4], vO[4], vE[4];
#pragma unroll
    for (int j = 0; j < 4; j++) {
      vC[j] = (64 * j2 + 32 * dw + 4 * j + g) * 1024 + ((m ^ (4 * j + g)) << 4) + BIAS - 1024 * j;
      const int row = 64 * dw + 8 * j + (lane >> 3);
      vO[j] = row * 1024 + j2 * 128 + (((lane & 7) ^ (((row >> 1) & 3) << 1)) << 4) + BIAS - 1024 * j;
      const int mm = 8 * (j & 1) + (lane >> 3);
      vE[j] = (64 * i2 + 32 * dw + 4 * (j >> 1) + 8 * (mm >> 2) + (mm & 3)) * 1024 + j2 * 128 + (((lane & 7) ^ (mm >> 1)) << 4) + BIAS - 1024 * j;
    }
    u32 vH = BIAS + WS_H + (2 * i2 + dw) * 32 * 1024 + lane * 16;
    u32 vV = BIAS + WS_V + ((2 * i2 + dw) * 48 + 1) * 1024 + lane * 16;
    WAITV(16);
    LBAR(); LSTAMP(6);
#pragma unroll
    for (int j = 0; j < 4; j++) { PIN(vC[j]); PIN(vO[j]); PIN(vE[j]); }
    PIN(vH); PIN(vV);
    __builtin_amdgcn_s_sleep(SLP6); issueW(sb + wslot(0), 0, 0); WAITV(12);
    LBAR(); LSTAMP(7); __builtin_amdgcn_s_sleep(SLP7); issueW(sb + wslot(1), 1, 0); WAITV(12);
    LBAR(); LSTAMP(8); issueW(sb + wslot(2), 2, 0); WAITV(12);
    LBAR(); LSTAMP(9); issueW(sb + wslot(3), 3, 0); WAITV(12);
    LBAR(); LSTAMP(10); issueZ(sb + RZ, 0); issueZ(sb + RZ + SZ, 1); WAITV(24);
    LBAR(); LSTAMP(11); issueA(sb, 2); dma4(sb + LX + dw * 4096, rX, 0, vE); WAITV(24);
    LBAR(); LSTAMP(12); issueZ(sb + RZ + 2 * SZ, 2); WAITV(24);
    na = sb + SA; nz = sb + RZ;
#pragma nounroll
    for (int s = 3; s < 16; s++) { LBAR(); LSTAMP(10 + s); issueZ(nz, s); issueA(na, s); rot(); WAITV(20); }
    auto frags = [&](u32 soff, u32 v) { dma4c(na + dw * 8192, rWS, soff, v); dma4c(na + dw * 8192 + 4096, rWS, soff + 4096, v); };
#pragma nounroll
    for (int s = 0; s < 4; s++) {
      LBAR(); LSTAMP(26 + s);
      frags((s ^ kx) * 8192, vH);
      rows(nz + dw * 8192, rC0, (s ^ kx) * 256, 16 * 1024, vC);
      rot(); WAITV(16);
    }
#pragma nounroll
    for (int s = 0; s < 4; s++) {
      LBAR(); LSTAMP(30 + s);
      frags(64 * 32 * 1024 + (s ^ kx) * 8192, vH);
      rows(nz + dw * 8192, rC1, (s ^ kx) * 256, 16 * 1024, vC);
      rot(); WAITV(16);
    }
#pragma nounroll
    for (int s = 0; s < 5; s++) {
      const int t = s < 4 ? s ^ (kx & KXV) : s;
      LBAR(); LSTAMP(34 + s);
      frags(t * 8192, vV);
      rows(nz + dw * 8192, rO, t * (128 * 1024), 32 * 1024, vO);
      rot(); WAITV(16);
    }
    LBAR(); LSTAMP(39);
    dma4c(na + dw * 8192, rWS, 5 * 8192, vV);
    dma4(nz + dw * 4096, rO, 5 * (128 * 1024) - dw * (32 * 1024), vO);
    WAITV(8);
    LBAR(); LSTAMP(40);
    WAITV(0);
    LBAR(); LSTAMP(41);
    asm volatile(".Lfused_end_l:");
    __builtin_amdgcn_endpgm();
  }
}
}

extern "C" void kernel_launch(void* const* d_in, const int* in_sizes, int n_in, void* d_out, int out_size, void* d_ws, size_t ws_size,
                              hipStream_t stream) {
  (void)hipFuncSetAttribute((const void*)fused_kernel, hipFuncAttributeMaxDynamicSharedMemorySize, LDS_BYTES);
  fused_kernel<<<256, 512, LDS_BYTES, stream>>>((const u16*)d_in[0], (const u16*)d_in[1], (const u16*)d_in[2], (const u16*)d_in[3],
                                                (const u16*)d_in[4], (const u16*)d_in[5], (const u16*)d_in[6], (const u16*)d_in[7],
                                                (const u16*)d_in[8], (const u16*)d_in[10], (const u16*)d_in[11], (const u16*)d_in[12],
                                                (const u16*)d_in[13], (const u16*)d_in[14], (float*)d_out, (char*)d_ws);
}
```

```cpp
#define OUTAUX 18
#include <hip/hip_runtime.h>
#include <stdint.h>

namespace {
typedef unsigned long long u64;
typedef unsigned short u16;
typedef unsigned u32;
typedef short bf16x8 __attribute__((ext_vector_type(8)));
typedef short s16x4 __attribute__((ext_vector_type(4)));
typedef float f32x4 __attribute__((ext_vector_type(4)));
typedef float f32x2 __attribute__((ext_vector_type(2)));
typedef __bf16 bf2 __attribute__((ext_vector_type(2)));
typedef int i32x4 __attribute__((ext_vector_type(4)));
typedef u32 u32x4 __attribute__((ext_vector_type(4)));
typedef u32 u32x2 __attribute__((ext_vector_type(2)));
#define LAS __attribute__((address_space(3)))
#define GAS __attribute__((address_space(1)))
#define DI __device__ __forceinline__

constexpr int FH = 688;
constexpr u32 SA = 16384, SZ = 24576, RZ = 3 * SA;
constexpr u32 L_GW = RZ + 3 * SZ, L_STAT = L_GW + 1024, L_GATE = L_STAT + 512, L_CARRY = L_GATE + 256, L_W0 = L_CARRY + 512;
constexpr u32 L_POW = L_W0 + 512;
constexpr u32 L_DUMMY = L_POW + 4096;
constexpr u32 LX = L_DUMMY + 256;
constexpr u32 LDS_BYTES = LX + 32768;
constexpr u32 uslot(int s) { return s == 1 ? RZ + 40960 : s == 2 ? RZ : LX; }
constexpr u32 wslot(int s) { return s == 1 ? RZ + SZ : s == 2 ? LX : RZ + 2 * SZ; }
constexpr u32 ring(int s, u32 sz) { return (s % 3) * sz; }

constexpr u32 WS_EPOCH = 0;
constexpr u32 WS_F1 = 4096, WS_F2 = WS_F1 + 8192, WS_F3 = WS_F2 + 8192;
constexpr u32 WS_STAT = 65536;
constexpr u32 WS_SEG = WS_STAT + 32 * 16 * 64 * 16;
constexpr u32 WS_Z = 1u << 20;
constexpr u32 WS_H = WS_Z + 64 * 48 * 1024;
constexpr u32 WS_V = WS_H + 2 * 64 * 32 * 1024;
constexpr u32 WS_END = WS_V + 64 * 48 * 1024;
constexpr u32 WS_STAMP = 16u << 20;
constexpr u64 SPIN_TICKS = 2000000;

constexpr u32 BIAS = 4096;
constexpr u32 OOB = 0x80000000u;
DI i32x4 mkrsrc(const void* p, u32 bytes) {
  u64 a = (u64)p - BIAS;
  i32x4 r;
  r[0] = __builtin_amdgcn_readfirstlane((u32)a);
  r[1] = __builtin_amdgcn_readfirstlane((u32)(a >> 32) & 0xffff);
  r[2] = bytes + BIAS;
  r[3] = 0x00020000;
  return r;
}
#define M0SET "s_mov_b32 m0, %0\n\ts_nop 0\n\t"
#define BL(v, o, f) "buffer_load_dwordx4 %" #v ", %1, %2 offen offset:" #o f " lds\n\t"
DI void dma1(u32 lds, i32x4 rs, u32 soff, u32 v0) {
  asm volatile(M0SET BL(3, 0, "")::"s"(lds), "s"(rs), "s"(soff), "v"(v0) : "memory");
}
DI void dma2(u32 lds, i32x4 rs, u32 soff, const u32* v) {
  asm volatile(M0SET BL(3, 0, "") BL(4, 1024, "")::"s"(lds), "s"(rs), "s"(soff), "v"(v[0]), "v"(v[1]) : "memory");
}
DI void dma4(u32 lds, i32x4 rs, u32 soff, const u32* v) {
  asm volatile(M0SET BL(3, 0, "") BL(4, 1024, "") BL(5, 2048, "") BL(6, 3072, "")::"s"(lds), "s"(rs), "s"(soff), "v"(v[0]), "v"(v[1]), "v"(v[2]), "v"(v[3]) : "memory");
}
DI void dma2c(u32 lds, i32x4 rs, u32 soff, u32 v0) {
  asm volatile(M0SET BL(3, 0, " sc1") BL(3, 1024, " sc1")::"s"(lds), "s"(rs), "s"(soff), "v"(v0) : "memory");
}
DI void dma4c(u32 lds, i32x4 rs, u32 soff, u32 v0) {
  asm volatile(M0SET BL(3, 0, " sc1") BL(3, 1024, " sc1") BL(3, 2048, " sc1") BL(3, 3072, " sc1")::"s"(lds), "s"(rs), "s"(soff), "v"(v0) : "memory");
}
DI void touch(u32 lds, const void* p) {
  asm volatile("s_mov_b32 m0, %0\n\ts_nop 0\n\tglobal_load_lds_dword %1, off" ::"s"(lds), "v"(p) : "memory");
}
DI u32x4 ld16_issue(u32 voff, i32x4 rs) {
  u32x4 r;
  asm volatile("buffer_load_dwordx4 %0, %1, %2, 0 offen sc1" : "=v"(r) : "v"(voff), "s"(rs) : "memory");
  return r;
}
DI u32x2 ld8_issue(u32 voff, i32x4 rs) {
  u32x2 r;
  asm volatile("buffer_load_dwordx2 %0, %1, %2, 0 offen sc1" : "=v"(r) : "v"(voff), "s"(rs) : "memory");
  return r;
}
#define WAITV(n) asm volatile("s_waitcnt vmcnt(%0)" ::"n"(n) : "memory")
#define WAITV1(n, a) asm volatile("s_waitcnt vmcnt(%1)" : "+v"(a) : "n"(n) : "memory")
#define WAITV4(n, a, b, c, d) asm volatile("s_waitcnt vmcnt(%4)" : "+v"(a), "+v"(b), "+v"(c), "+v"(d) : "n"(n) : "memory")
#define WAITV8(a) asm volatile("s_waitcnt vmcnt(0)" : "+v"(a[0]), "+v"(a[1]), "+v"(a[2]), "+v"(a[3]), "+v"(a[4]), "+v"(a[5]), "+v"(a[6]), "+v"(a[7])::"memory")
#define LBAR() asm volatile("s_waitcnt lgkmcnt(0)\n\ts_barrier" ::: "memory")
#define PIN(a) asm volatile("" : "+v"(a))
#define MF(c, a, b) asm volatile("v_mfma_f32_16x16x32_bf16 %0, %1, %2, %0" : "+v"(c) : "v"(a), "v"(b))
#define MF0(c, a, b) asm volatile("v_mfma_f32_16x16x32_bf16 %0, %1, %2, 0" : "=&v"(c) : "v"(a), "v"(b))
#define MFK(first, c, a, b) do { if (first) MF0(c, a, b); else MF(c, a, b); } while (0)
#define MFDONE4(a, b, c, d) asm volatile("s_nop 7\n\ts_nop 1" : "+v"(a), "+v"(b), "+v"(c), "+v"(d))
#define MFDONE6(a, b, c, d, e, f) asm volatile("s_nop 7\n\ts_nop 1" : "+v"(a), "+v"(b), "+v"(c), "+v"(d), "+v"(e), "+v"(f))

DI bf16x8 lds16(u32 a) { return *(const LAS bf16x8*)(size_t)a; }
DI f32x4 ldsf4(u32 a) { return *(const LAS f32x4*)(size_t)a; }
DI f32x2 ldsf2(u32 a) { return *(const LAS f32x2*)(size_t)a; }
DI s16x4 lds8(u32 a) { return *(const LAS s16x4*)(size_t)a; }
DI s16x4 ldstr(u32 a) { return __builtin_amdgcn_ds_read_tr16_b64_v4i16((LAS s16x4*)(size_t)a); }
DI bf16x8 cat(s16x4 lo, s16x4 hi) { return __builtin_shufflevector(lo, hi, 0, 1, 2, 3, 4, 5, 6, 7); }
DI bf16x8 ldtr2(u32 a, u32 d) { return cat(ldstr(a), ldstr(a + d)); }
DI float bf2f(u16 v) { return __uint_as_float((u32)v << 16); }
DI u32 pk(f32x2 v) { return __builtin_bit_cast(u32, __builtin_convertvector(v, bf2)); }
DI f32x2 unpk(u32 v) { return f32x2{__uint_as_float(v << 16), __uint_as_float(v & 0xffff0000u)}; }
DI f32x2 fma2(f32x2 a, f32x2 b, f32x2 c) { return __builtin_elementwise_fma(a, b, c); }
DI f32x2 sp(float a) { return f32x2{a, a}; }
DI f32x2 half(f32x4 v, int h) { return h ? v.zw : v.xy; }
DI f32x2 gelu2(f32x2 x) {
  f32x2 a = x * fma2(sp(-0.1029432397f), x * x, sp(-2.302208198f));
  f32x2 e = f32x2{__builtin_amdgcn_exp2f(a[0]), __builtin_amdgcn_exp2f(a[1])} + 1.0f;
  return x * f32x2{__builtin_amdgcn_rcpf(e[0]), __builtin_amdgcn_rcpf(e[1])};
}
struct cf { float re, im; };
DI float add1(float a, float b) { float r; asm("v_add_f32 %0, %1, %2" : "=v"(r) : "v"(a), "v"(b)); return r; }
DI float mul1(float a, float b) { float r; asm("v_mul_f32 %0, %1, %2" : "=v"(r) : "v"(a), "v"(b)); return r; }
DI float fma1(float a, float b, float c) { float r; asm("v_fma_f32 %0, %1, %2, %3" : "=v"(r) : "v"(a), "v"(b), "v"(c)); return r; }
DI float fnma1(float a, float b, float c) { float r; asm("v_fma_f32 %0, -%1, %2, %3" : "=v"(r) : "v"(a), "v"(b), "v"(c)); return r; }
DI f32x2 fma2h(f32x2 a, f32x2 b, f32x2 c) { f32x2 r; asm("v_pk_fma_f32 %0, %1, %2, %3 op_sel:[1,0,0]" : "=v"(r) : "v"(a), "v"(b), "v"(c)); return r; }
DI f32x2 fnma2h(f32x2 a, f32x2 b, f32x2 c) {
  f32x2 r;
  asm("v_pk_fma_f32 %0, %1, %2, %3 op_sel:[1,0,0] neg_lo:[1,0,0] neg_hi:[1,0,0]" : "=v"(r) : "v"(a), "v"(b), "v"(c));
  return r;
}
DI cf cmul(cf a, cf b) { return {fnma1(a.im, b.im, mul1(a.re, b.re)), fma1(a.im, b.re, mul1(a.re, b.im))}; }
DI cf cfma(cf a, cf b, cf c) { return {fma1(a.re, b.re, fnma1(a.im, b.im, c.re)), fma1(a.re, b.im, fma1(a.im, b.re, c.im))}; }
DI void sincos_cw(float x, float& s, float& c) {
  float k = __builtin_rintf(x * 0.6366197724f);
  float r = __builtin_fmaf(k, -1.57079637050628662109375f, x);
  r = __builtin_fmaf(k, 4.37113900018624283e-8f, r);
  float r2 = r * r;
  float ps = __builtin_fmaf(r2, __builtin_fmaf(r2, __builtin_fmaf(r2, 2.7557314297e-06f, -1.9841270114e-04f), 8.3333337680e-03f), -1.6666667163e-01f);
  float sr = __builtin_fmaf(r * r2, ps, r);
  float pc = __builtin_fmaf(r2, __builtin_fmaf(r2, __builtin_fmaf(r2, -2.7557314297e-07f, 2.4801587642e-05f), -1.3888889225e-03f), 4.1666667908e-02f);
  float cr = __builtin_fmaf(r2 * r2, pc, __builtin_fmaf(r2, -0.5f, 1.0f));
  int q = (int)k;
  float ss = (q & 1) ? cr : sr, cc = (q & 1) ? sr : cr;
  s = (q & 2) ? -ss : ss;
  c = ((q + 1) & 2) ? -cc : cc;
}
template <int CTRL>
DI float dpp(float v) {
  return __int_as_float(__builtin_amdgcn_update_dpp(0, __float_as_int(v), CTRL, 0xf, 0xf, true));
}
DI float reduce8(const f32x2* v, int m) {
  float a[4], b[2];
  const bool h3 = m & 8, h2 = m & 4, h1 = m & 2;
#pragma unroll
  for (int k = 0; k < 4; k++) {
    const float lo = v[k >> 1][k & 1], hi = v[2 + (k >> 1)][k & 1];
    a[k] = (h3 ? hi : lo) + dpp<0x140>(h3 ? lo : hi);
  }
#pragma unroll
  for (int k = 0; k < 2; k++) b[k] = (h2 ? a[k + 2] : a[k]) + dpp<0x141>(h2 ? a[k] : a[k + 2]);
  float c = (h1 ? b[1] : b[0]) + dpp<0x4E>(h1 ? b[0] : b[1]);
  return c + dpp<0xB1>(c);
}
DI void rows4(float v, float* o) {
  auto s = __builtin_amdgcn_permlane16_swap(__float_as_uint(v), __float_as_uint(v), false, false);
  auto e = __builtin_amdgcn_permlane32_swap(s[0], s[0], false, false);
  auto d = __builtin_amdgcn_permlane32_swap(s[1], s[1], false, false);
  o[0] = __uint_as_float(e[0]); o[1] = __uint_as_float(d[0]); o[2] = __uint_as_float(e[1]); o[3] = __uint_as_float(d[1]);
}
DI float halves(float v) {
  auto s = __builtin_amdgcn_permlane32_swap(__float_as_uint(v), __float_as_uint(v), false, false);
  return __uint_as_float(s[0]) + __uint_as_float(s[1]);
}
DI bool none(u32 d) { PIN(d); return __builtin_amdgcn_ballot_w64(d != 0) == 0; }
DI u32 tagdiff(u32x4 v, u32 lo, u32 hi) { return (v[1] ^ lo) | (v[3] ^ hi); }
DI bool flags_ok(u32x4 v, u32 lo, u32 hi, bool need) { return none(need ? (v[0] ^ lo) | (v[1] ^ hi) | (v[2] ^ lo) | (v[3] ^ hi) : 0u); }
DI void spin_flags(u32 voff, i32x4 rs, u32 lo, u32 hi, bool need) {
  const u64 t0 = __builtin_amdgcn_s_memrealtime();
  for (;;) {
    u32x4 v = ld16_issue(voff, rs);
    WAITV1(0, v);
    if (flags_ok(v, lo, hi, need) || __builtin_amdgcn_s_memrealtime() - t0 > SPIN_TICKS) break;
    __builtin_amdgcn_s_sleep(2);
  }
}
DI void put_flag(char* ws, u32 off, u64 tag, int lane) {
  WAITV(0);
  if (lane == 0) __hip_atomic_store((GAS u64*)(ws + off), tag, __ATOMIC_RELAXED, __HIP_MEMORY_SCOPE_AGENT);
}
DI u64 next_tag(u32 lo, u32 hi) {
  return (((u64)(u32)__builtin_amdgcn_readfirstlane(hi) << 32) | (u32)__builtin_amdgcn_readfirstlane(lo)) * 6364136223846793005ull + 1442695040888963407ull;
}
#define STAT_BAR 7
#define SLP6 3
#define SLP7 7
#define P2S 7
#define KXG0 3
#define KXV 3
#define KQ ((xcd & 3) * 4)
#define PRIO_D 0
#define PRIO_A 3
#define PRIO_C 2
#define LOADER_PRIO __builtin_amdgcn_s_setprio(PRIO_D)
#define MFMA_PRIO __builtin_amdgcn_s_setprio(PRIO_C)
DI void try_flag(bool& done, bool force, char* ws, u32 off, u64 tag, int lane) {
  if (!done) {
    u32 st;
    asm volatile("s_getreg_b32 %0, hwreg(HW_REG_IB_STS)" : "=s"(st));
    if (force || (st & 0x00C0000Fu) == 0) { put_flag(ws, off, tag, lane); done = true; }
  }
}
#define STAMP(k)
#define LSTAMP(k) STAMP(k)

__global__ __launch_bounds__(512, 2) void fused_kernel(const u16* x, const u16* lre, const u16* lim, const u16* bre, const u16* bim,
                                                       const u16* cre, const u16* cim, const u16* ldt, const u16* dsk, const u16* win,
                                                       const u16* wsp, const u16* bsp, const u16* wout, const u16* gw, float* out, char* ws) {
  extern __shared__ __attribute__((aligned(16))) char smem[];
  const int tid = threadIdx.x, wave = __builtin_amdgcn_readfirstlane(tid >> 6), lane = tid & 63;
  const int xcd = blockIdx.x & 7, idx = blockIdx.x >> 3;
  const int i2 = (xcd & 3) * 8 + (idx & 7), j2 = (xcd >> 2) * 4 + (idx >> 3);
  const bool narrow = (j2 & 3) == 3;
  const int tb = 6 * j2 - 2 * (j2 >> 2);
  const u32 sb = (u32)(size_t)(LAS char*)smem;
  const int m = lane & 15, g = lane >> 4;

  u64 code;
  int code_len;
  asm volatile("s_getpc_b64 %0\n.Lfused_begin:\n\ts_mov_b32 %1, .Lfused_end_c-.Lfused_begin\n\ts_max_i32 %1, %1, .Lfused_end_l-.Lfused_begin" : "=s"(code), "=s"(code_len)::"scc");

  const int kx = (i2 + j2) & 3;
  const int kq = KQ;

  if (wave >= 4) {
    MFMA_PRIO;
    const int cw = wave - 4, wr = cw >> 1, wc = cw & 1, qq = m >> 2, p = lane & 3;
    const __amdgpu_buffer_rsrc_t rS = __builtin_amdgcn_make_buffer_rsrc(ws, 0, WS_END, 0x00020000);
    const u64 ep = __hip_atomic_load((GAS u64*)(ws + WS_EPOCH), __ATOMIC_RELAXED, __HIP_MEMORY_SCOPE_AGENT);
    const u64 tag = next_tag((u32)ep, (u32)(ep >> 32));
    const u32 tlo = (u32)tag, thi = (u32)(tag >> 32);
    const int tile0 = tb + 3 * wc;
    auto valid = [&](int n) { return tile0 + n >= 1 && !(narrow && wc == 1 && n >= 1); };
    const int rg = 2 * i2 + wr;

    u32 aR[4], aW[3];
#pragma unroll
    for (int ks = 0; ks < 4; ks++) aR[ks] = sb + m * 256 + (((4 * ks + g) ^ m) << 4);
#pragma unroll
    for (int n = 0; n < 3; n++) aW[n] = sb + RZ + (8 * g + qq) * 192 + (((3 * wc + n) ^ (g & 1)) << 5) + p * 8;
    auto advR = [&](u32 d) {
#pragma unroll
      for (int ks = 0; ks < 4; ks++) { aR[ks] += d; PIN(aR[ks]); }
    };
    auto advW = [&](u32 d) {
#pragma unroll
      for (int n = 0; n < 3; n++) { aW[n] += d; PIN(aW[n]); }
    };
    auto step = [](int st, u32 sz) -> u32 { return ring(st, sz) - ring(st - 1, sz); };

    bf16x8 xf[2][16];
    bf16x8 wf[2][3];
    f32x4 z2[2][3];
    advR(32 * wr * 256);
    __builtin_amdgcn_s_waitcnt(0xC07F);
    LBAR();
    xf[0][0] = lds16(aR[0]);
#pragma unroll
    for (int n = 0; n < 3; n++) wf[0][n] = ldtr2(aW[n], 768);
    xf[1][0] = lds16(aR[0] + 4096);
#pragma unroll
    for (int kk = 0; kk < 16; kk++) {
      const int k1 = kk + 1, ks1 = k1 & 3;
      const bool nx = kk < 15;
      bf16x8* cur = wf[kk & 1];
      bf16x8* nxt = wf[k1 & 1];
      if (nx && ks1 == 0) { LBAR(); advR(step(k1 >> 2, SA)); advW(step(k1 >> 2, SZ)); }
      MFK(kk == 0, z2[0][0], xf[0][kk], cur[0]);
      if (nx) { xf[0][k1] = lds16(aR[ks1]); nxt[0] = ldtr2(aW[0] + ks1 * 6144, 768); }
      MFK(kk == 0, z2[0][1], xf[0][kk], cur[1]);
      if (nx) nxt[1] = ldtr2(aW[1] + ks1 * 6144, 768);
      MFK(kk == 0, z2[0][2], xf[0][kk], cur[2]);
      if (nx) nxt[2] = ldtr2(aW[2] + ks1 * 6144, 768);
      MFK(kk == 0, z2[1][0], xf[1][kk], cur[0]);
      if (nx) xf[1][k1] = lds16(aR[ks1] + 4096);
      MFK(kk == 0, z2[1][1], xf[1][kk], cur[1]);
      MFK(kk == 0, z2[1][2], xf[1][kk], cur[2]);
    }
    MFDONE6(z2[0][0], z2[0][1], z2[0][2], z2[1][0], z2[1][1], z2[1][2]);
    STAMP(0);
    u32 z2p[3][4];
    {
      if (tile0 == 0) {
        z2[0][0] = f32x4{0.f, 0.f, 0.f, 0.f};
        z2[1][0] = f32x4{0.f, 0.f, 0.f, 0.f};
        PIN(z2[0][0]);
      }
      f32x2 s1[4], s2[4];
#pragma unroll
      for (int n = 0; n < 3; n++)
#pragma unroll
        for (int k = 0; k < 4; k++) {
          const f32x2 v = gelu2(half(z2[k >> 1][n], k & 1));
          s1[k] = n ? s1[k] + v : v;
          s2[k] = n ? fma2(v, v, s2[k]) : v * v;
          z2p[n][k] = pk(v);
        }
      const float r1 = reduce8(s1, m), r2 = reduce8(s2, m);
      if (!(m & 1))
        __builtin_amdgcn_raw_buffer_store_b128(u32x4{__float_as_uint(r1), tlo, __float_as_uint(r2), thi}, rS,
                                               WS_STAT + ((i2 * 16 + 2 * j2 + wc) * 64 + 32 * wr + 8 * g + (m >> 1)) * 16, 0, 16);
    }
    STAMP(1);
    f32x4 rs0, rs1, sh0, sh1;
    auto norm = [&](int n) {
      const u32 as = sb + L_STAT + (32 * wr + 8 * g) * 4;
      if (n == 0) { rs0 = ldsf4(as); rs1 = ldsf4(as + 16); sh0 = ldsf4(as + 256); sh1 = ldsf4(as + 272); }
      u32x4 o;
#pragma unroll
      for (int k = 0; k < 4; k++) o[k] = pk(fma2(unpk(z2p[n][k]), half(k >> 1 ? rs1 : rs0, k & 1), half(k >> 1 ? sh1 : sh0, k & 1)));
      if (n == 0 && tile0 == 0) o = u32x4{0u, 0u, 0u, 0u};
      __builtin_amdgcn_raw_buffer_store_b128(o, rS, n == 0 || valid(n) ? WS_Z + (rg * 48 + tile0 + n) * 1024 + lane * 16 : OOB, 0, 16);
      if (n == 2) STAMP(5);
    };
    bool pub1 = false;
    float gt[2];
    const u32 f1 = WS_F1 + ((j2 * 32 + i2) * 4 + cw) * 8;
    f32x4 ur[2][2], ui[2][2];
    {
      bf16x8 bf[2][4];
      advR(uslot(0) + 32 * (wc - wr) * 256);
      LBAR();
#pragma unroll
      for (int c = 0; c < 4; c++) bf[0][c] = lds16(aR[0] + (c >> 1) * 16384 + (c & 1) * 4096);
#pragma unroll
      for (int kk = 0; kk < 16; kk++) {
        const int k1 = kk + 1, ks1 = k1 & 3;
        const bool nx = kk < 15;
        bf16x8* cur = bf[kk & 1];
        bf16x8* nxt = bf[k1 & 1];
        if (kk == 11) {
          f32x4 ga[2];
          bf16x8 gf[2];
          u32 aG = sb + L_GW + g * 16 + kx * 256;
          PIN(aG);
          gf[0] = lds16(aG);
#pragma unroll
          for (int q = 0; q < 16; q++) {
            if ((q & 3) == 3 && q < 15) { aG ^= (((q + 1) >> 2) ^ (q >> 2)) * 256; PIN(aG); }
            if (q < 15) gf[(q + 1) & 1] = lds16(aG + ((q + 1) & 3) * 64);
            MFK(q == 0, ga[0], gf[q & 1], xf[0][q]);
            MFK(q == 0, ga[1], gf[q & 1], xf[1][q]);
          }
          asm volatile("s_nop 7\n\ts_nop 1" : "+v"(ga[0]), "+v"(ga[1]));
#pragma unroll
          for (int u = 0; u < 2; u++) { gt[u] = __builtin_amdgcn_rcpf(1.0f + __expf(-ga[u][0])); PIN(gt[u]); }
        }
        if (nx && ks1 == 0) { LBAR(); advR(uslot(k1 >> 2) - uslot((k1 >> 2) - 1)); }
        if (kk == 11) { norm(0); norm(1); norm(2); }
        if (kk >= 13 && !pub1) { try_flag(pub1, false, ws, f1, tag, lane); if (pub1) STAMP(7); }
#pragma unroll
        for (int c = 0; c < 4; c++) {
          if (c < 2) MFK(kk == 0, ur[0][c], xf[0][kk], cur[c]); else MFK(kk == 0, ui[0][c - 2], xf[0][kk], cur[c]);
          if (nx) nxt[c] = lds16(aR[ks1] + (c >> 1) * 16384 + (c & 1) * 4096);
        }
#pragma unroll
        for (int c = 0; c < 4; c++) {
          if (c < 2) MFK(kk == 0, ur[1][c], xf[1][kk], cur[c]); else MFK(kk == 0, ui[1][c - 2], xf[1][kk], cur[c]);
        }
      }
    }
    MFDONE4(ur[1][0], ur[1][1], ui[1][0], ui[1][1]);
    STAMP(2);
    cf pre[2], mul[2], wend[2];
    const u32 ap = sb + L_POW + (32 * wc + m) * 16;
    {
      f32x4 t0[2], t1[2];
      f32x2 er[2], ei[2];
      cf e[2];
#pragma unroll
      for (int a = 0; a < 2; a++) { t0[a] = ldsf4(ap + a * 256); t1[a] = ldsf4(ap + a * 256 + 1024); er[a] = ur[0][a].xy; ei[a] = ui[0][a].xy; }
      if (!pub1) { try_flag(pub1, false, ws, f1, tag, lane); if (pub1) STAMP(7); }
#pragma unroll
      for (int k = 1; k < 4; k++)
#pragma unroll
        for (int a = 0; a < 2; a++) {
          const f32x2 nr = fma2h(t0[a].xy, er[a], fnma2h(t0[a].zw, ei[a], half(ur[k >> 1][a], k & 1)));
          const f32x2 ni = fma2h(t0[a].xy, ei[a], fma2h(t0[a].zw, er[a], half(ui[k >> 1][a], k & 1)));
          er[a] = nr; ei[a] = ni;
        }
      float xr[2][4], xi[2][4];
#pragma unroll
      for (int a = 0; a < 2; a++) {
        e[a] = cfma(cf{t0[a][0], t0[a][2]}, cf{er[a][0], ei[a][0]}, cf{er[a][1], ei[a][1]});
        rows4(e[a].re, xr[a]);
        rows4(e[a].im, xi[a]);
      }
#pragma unroll
      for (int a = 0; a < 2; a++) {
        const cf l8 = {t1[a][0], t1[a][1]}, l16 = {t1[a][2], t1[a][3]};
        const cf e0 = {xr[a][0], xi[a][0]}, e1 = cfma(l8, e0, cf{xr[a][1], xi[a][1]}), e2 = cfma(l8, e1, cf{xr[a][2], xi[a][2]});
        wend[a] = cfma(l8, e2, cf{xr[a][3], xi[a][3]});
        const cf lo = (g & 1) ? e0 : cf{0.f, 0.f}, hi = (g & 1) ? e2 : e1;
        pre[a] = (g & 2) ? hi : lo;
        const cf q1 = (g & 1) ? l8 : cf{1.f, 0.f}, q2 = (g & 2) ? l16 : cf{1.f, 0.f};
        mul[a] = cmul(q1, q2);
      }
      PIN(pre[0].re); PIN(pre[0].im); PIN(mul[0].re); PIN(mul[0].im); PIN(pre[1].re); PIN(pre[1].im); PIN(mul[1].re); PIN(mul[1].im);
      if (!pub1) { try_flag(pub1, true, ws, f1, tag, lane); STAMP(7); }
    }
    if (wr == 0 && g < 2)
      *(LAS f32x2*)(size_t)(sb + L_W0 + (32 * wc + lane) * 8) = g ? f32x2{wend[1].re, wend[1].im} : f32x2{wend[0].re, wend[0].im};
    STAMP(3);
    f32x4 z1[2][3];
    u32 z1p[2][3][2];
    {
      advW(wslot(0) - RZ);
      LBAR();
      STAMP(4);
#pragma unroll
      for (int n = 0; n < 3; n++) wf[0][n] = ldtr2(aW[n], 768);
#pragma unroll
      for (int kk = 0; kk < 16; kk++) {
        const int k1 = kk + 1, ks1 = k1 & 3;
        const bool nx = kk < 15;
        bf16x8* cur = wf[kk & 1];
        bf16x8* nxt = wf[k1 & 1];
        if (nx && ks1 == 0) { LBAR(); advW(wslot(k1 >> 2) - wslot((k1 >> 2) - 1)); }
        if (kk == 1 && wr == 1) {
          cf e[2];
#pragma unroll
          for (int a = 0; a < 2; a++) {
            const f32x2 w0 = ldsf2(sb + L_W0 + (32 * wc + 16 * a + m) * 8);
            const f32x4 t2 = ldsf4(ap + a * 256 + 2048);
            const cf l32 = {t2[2], t2[3]};
            e[a] = cfma(l32, cf{w0[0], w0[1]}, wend[a]);
            pre[a] = cfma(mul[a], cf{w0[0], w0[1]}, pre[a]);
            mul[a] = cmul(mul[a], l32);
          }
          if (g < 2)
            __builtin_amdgcn_raw_buffer_store_b128(u32x4{__float_as_uint(g ? e[1].re : e[0].re), tlo, __float_as_uint(g ? e[1].im : e[0].im), thi}, rS,
                                                   WS_SEG + (j2 * 32 + i2) * 1024 + (32 * wc + lane) * 16, 0, 16);
        }
#pragma unroll
        for (int n = 0; n < 3; n++) {
          MFK(kk == 0, z1[0][n], cur[n], xf[0][kk]);
          if (nx) nxt[n] = ldtr2(aW[n] + ks1 * 6144, 768);
        }
#pragma unroll
        for (int n = 0; n < 3; n++) MFK(kk == 0, z1[1][n], cur[n], xf[1][kk]);
      }
    }
    STAMP(6);
    f32x4 ga[2][3] = {}, ya[2][2];
    const int dcol = 64 * j2 + 32 * wc + 4 * g;
    u32x2 de[2];
#pragma unroll
    for (int n = 0; n < 2; n++) de[n] = *(const u32x2*)(dsk + dcol + 16 * n);
    const int trow = 64 * i2 + 32 * wr + 8 * (m >> 2) + (m & 3);
    const float bias0 = bf2f(bsp[trow]), bias1 = bf2f(bsp[trow + 4]);
    {
      u32 aZ = sb + RZ + 3 * wc * 1024 + lane * 16;
      bf16x8 fa[2][3], fb[2][2];
      advR(SA - uslot(3) + 32 * (wr - wc) * 256);
      asm volatile("" : "+v"(ga[0][0]), "+v"(ga[0][1]), "+v"(ga[0][2]), "+v"(ga[1][0]), "+v"(ga[1][1]), "+v"(ga[1][2]));
      LBAR();
      STAMP(8);
      fb[0][0] = lds16(aR[0]);
#pragma unroll
      for (int n = 0; n < 3; n++) fa[0][n] = lds16(aZ + n * 1024);
      fb[0][1] = lds16(aR[0] + 4096);
      auto group = [&](int ks, bool nx) {
        const int c = ks & 1, ks1 = (ks + 1) & 3;
        MF(ga[0][0], fa[c][0], fb[c][0]);
        if (nx) { fb[c ^ 1][0] = lds16(aR[ks1]); fa[c ^ 1][0] = lds16(aZ + ks1 * 6144); }
        MF(ga[0][1], fa[c][1], fb[c][0]);
        if (nx) fa[c ^ 1][1] = lds16(aZ + ks1 * 6144 + 1024);
        MF(ga[0][2], fa[c][2], fb[c][0]);
        if (nx) fa[c ^ 1][2] = lds16(aZ + ks1 * 6144 + 2048);
        MF(ga[1][0], fa[c][0], fb[c][1]);
        if (nx) fb[c ^ 1][1] = lds16(aR[ks1] + 4096);
        MF(ga[1][1], fa[c][1], fb[c][1]);
        MF(ga[1][2], fa[c][2], fb[c][1]);
      };
      bool pub = false;
      u32x4 hout[2];
#pragma unroll
      for (int s = 0; s < 16; s++) {
        group(0, true);
        group(1, true);
        group(2, true);
#pragma unroll
        for (int n = 0; n < 3; n++)
          if (s == (n == 0 ? 10 : 11)) {
#pragma unroll
            for (int u = 0; u < 2; u++) PIN(z1[u][n]);
#pragma unroll
            for (int k = 0; k < 4; k++) { z1p[k >> 1][n][k & 1] = pk(gelu2(half(z1[k >> 1][n], k & 1))); PIN(z1p[k >> 1][n][k & 1]); }
          }
        if (s >= P2S + 1 && s <= P2S + 2) try_flag(pub, s == P2S + 2, ws, WS_F2 + ((i2 * 8 + j2) * 4 + cw) * 8, tag, lane);
#pragma unroll
        for (int a = 0; a < 2; a++) {
          if (false) {
            const u32 off = WS_H + (rg * 32 + 4 * j2 + 2 * wc + a) * 1024 + (m * 4 + g) * 16;
#pragma unroll
            for (int c = 0; c < 2; c++) __builtin_amdgcn_raw_buffer_store_b128(hout[c], rS, off + c * (64 * 32 * 1024), 0, 16);
          }
        }
#pragma unroll
        for (int a = 0; a < 2; a++) {
          if (s == 3 + 4 * a) {
            PIN(ur[0][a]); PIN(ur[1][a]); PIN(ui[0][a]); PIN(ui[1][a]);
            const f32x2 cin = ldsf2(sb + L_CARRY + (32 * wc + 16 * a + m) * 8);
            const f32x4 t0 = ldsf4(ap + a * 256), t2 = ldsf4(ap + a * 256 + 2048);
            const cf h0 = cfma(mul[a], cf{cin[0], cin[1]}, pre[a]);
            f32x2 hr = sp(h0.re), hi = sp(h0.im);
            const f32x2 cr = sp(t2[0]), ci = sp(-t2[1]);
            u32 o[2][4];
#pragma unroll
            for (int k = 0; k < 4; k++) {
              f32x2 pr = half(ur[k >> 1][a], k & 1), pi = half(ui[k >> 1][a], k & 1);
              const cf w = cfma(cf{t0[0], t0[2]}, cf{pr[0], pi[0]}, cf{pr[1], pi[1]});
              pr[1] = w.re; pi[1] = w.im;
              const f32x2 nr = fma2h(hr, t0.xy, fnma2h(hi, t0.zw, pr));
              const f32x2 ni = fma2h(hi, t0.xy, fma2h(hr, t0.zw, pi));
              hr = nr; hi = ni;
              o[0][k] = pk(fma2(cr, hr, ci * hi));
              o[1][k] = pk(fma2(-cr, hi, ci * hr));
            }
            const bool sw = m & 8;
#pragma unroll
            for (int c = 0; c < 2; c++) {
              hout[c] = sw ? u32x4{o[c][2], o[c][3], o[c][0], o[c][1]} : u32x4{o[c][0], o[c][1], o[c][2], o[c][3]};
              __builtin_amdgcn_raw_buffer_store_b128(hout[c], rS, WS_H + (rg * 32 + 4 * j2 + 2 * wc + a) * 1024 + (m * 4 + g) * 16 + c * (64 * 32 * 1024), 0, 16);
            }
          }
        }
        if (s == 12) {
#pragma unroll
          for (int u = 0; u < 2; u++)
#pragma unroll
            for (int n = 0; n < 2; n++) {
              const u32x2 xe = *(const LAS u32x2*)(size_t)(sb + LX + (32 * wr + 16 * u + m) * 128 + (((4 * wc + 2 * n + (g >> 1)) ^ (m >> 1)) << 4) + (g & 1) * 8);
              const f32x2 t0 = unpk(xe[0]) * unpk(de[n][0]), t1 = unpk(xe[1]) * unpk(de[n][1]);
              ya[u][n] = f32x4{t0[0], t0[1], t1[0], t1[1]};
              PIN(ya[u][n]);
            }
        }
        if (s < 15) {
          LBAR();
          advR(step(s + 2, SA));
          aZ += step(s + 1, SZ);
          PIN(aZ);
        }
        group(3, s < 15);
      }
    }
    MFDONE6(ga[0][0], ga[0][1], ga[0][2], ga[1][0], ga[1][1], ga[1][2]);
    STAMP(9);
#pragma unroll
    for (int n = 0; n < 3; n++) {
      u32 h[2][2];
#pragma unroll
      for (int u = 0; u < 2; u++)
#pragma unroll
        for (int k = 0; k < 2; k++) h[u][k] = pk(unpk(z1p[u][n][k]) * (half(ga[u][n], k) + (u ? bias1 : bias0)));
      u32x4 o = (g & 1) ? u32x4{h[1][0], h[1][1], h[0][0], h[0][1]} : u32x4{h[0][0], h[0][1], h[1][0], h[1][1]};
      if (tile0 + n == 44) o = u32x4{0u, 0u, 0u, 0u};
      __builtin_amdgcn_raw_buffer_store_b128(o, rS, valid(n) || tile0 + n == 44 ? WS_V + (rg * 48 + tile0 + n) * 1024 + lane * 16 : OOB, 0, 16);
    }
    STAMP(10);
    f32x4 yb[2][2] = {};
    {
      u32 aH[2];
#pragma unroll
      for (int u = 0; u < 2; u++) aH[u] = sb + 2 * SA + (wr * 8 + (g >> 1)) * 1024 + (8 * (g & 1) + qq) * 64 + p * 16 + ((u ^ (g & 1)) << 3);
      bf16x8 fa[2][2], fb[2][2];
      advR(RZ + SZ - SA + 32 * (wc - wr) * 256);
      asm volatile("" : "+v"(ya[0][0]), "+v"(ya[0][1]), "+v"(ya[1][0]), "+v"(ya[1][1]));
      LBAR();
      fb[0][0] = ldtr2(aH[0], 256);
      fa[0][0] = lds16(aR[0]);
      fa[0][1] = lds16(aR[0] + 4096);
      fb[0][1] = ldtr2(aH[1], 256);
      auto group = [&](int ks, bool nx) {
        const int c = ks & 1, ks1 = (ks + 1) & 3;
        MF(ya[0][0], fa[c][0], fb[c][0]);
        if (nx) { fb[c ^ 1][0] = ldtr2(aH[0] + ks1 * 2048, 256); fa[c ^ 1][0] = lds16(aR[ks1]); }
        MF(ya[0][1], fa[c][1], fb[c][0]);
        if (nx) { fa[c ^ 1][1] = lds16(aR[ks1] + 4096); fb[c ^ 1][1] = ldtr2(aH[1] + ks1 * 2048, 256); }
        MF(ya[1][0], fa[c][0], fb[c][1]);
        MF(ya[1][1], fa[c][1], fb[c][1]);
      };
      int slot = 1;
      bool pub = false;
#pragma nounroll
      for (int s = 0; s < 7; s++) {
        group(0, true);
        group(1, true);
        group(2, true);
        if (!pub) { try_flag(pub, s == 2, ws, WS_F3 + ((i2 * 8 + j2) * 4 + cw) * 8, tag, lane); if (pub) STAMP(15); }
        LBAR();
        const u32 da = slot == 1 ? -2 * SA : SA, dz = slot == 2 ? -2 * SZ : SZ;
        slot = slot == 2 ? 0 : slot + 1;
        advR(dz);
        aH[0] += da; aH[1] += da;
        group(3, true);
      }
      group(0, true);
      group(1, true);
      group(2, true);
      group(3, false);
    }
    STAMP(11);
    {
      u32 aV[2], aV2[2], aO[2];
#pragma unroll
      for (int u = 0; u < 2; u++) {
        aV[u] = sb + SA + wr * 8192 + lane * 16 + ((u ^ (g & 1)) << 3);
        aV2[u] = aV[u] + 1024;
        PIN(aV2[u]);
        aO[u] = sb + RZ + (4 * g + qq) * 128 + (((2 * wc + u) ^ (2 * (g & 1) + (qq >> 1))) << 5) + p * 8;
      }
      bf16x8 fa[2][2], fb[2][2];
      auto rdV = [&](int u, int ks) { return cat(lds8(aV[u] + ks * 2048), lds8(aV2[u] + ks * 2048)); };
      asm volatile("" : "+v"(yb[0][0]), "+v"(yb[0][1]), "+v"(yb[1][0]), "+v"(yb[1][1]));
      LBAR();
      fb[0][0] = rdV(0, 0);
      fa[0][0] = ldtr2(aO[0], 2048);
      fa[0][1] = ldtr2(aO[1], 2048);
      fb[0][1] = rdV(1, 0);
      auto group = [&](int ks, bool nx) {
        const int c = ks & 1, ks1 = (ks + 1) & 3;
        MF(yb[0][0], fa[c][0], fb[c][0]);
        if (nx) { fb[c ^ 1][0] = rdV(0, ks1); fa[c ^ 1][0] = ldtr2(aO[0] + ks1 * 4096, 2048); }
        MF(yb[0][1], fa[c][1], fb[c][0]);
        if (nx) { fa[c ^ 1][1] = ldtr2(aO[1] + ks1 * 4096, 2048); fb[c ^ 1][1] = rdV(1, ks1); }
        MF(yb[1][0], fa[c][0], fb[c][1]);
        MF(yb[1][1], fa[c][1], fb[c][1]);
      };
      int slot = 0;
#pragma nounroll
      for (int s = 0; s < 5; s++) {
        group(0, true);
        group(1, true);
        group(2, true);
        LBAR();
        const u32 da = slot == 1 ? -2 * SA : SA, dz = slot == 2 ? -2 * SZ : SZ;
        slot = slot == 2 ? 0 : slot + 1;
        aV[0] += da; aV[1] += da; aV2[0] += da; aV2[1] += da; PIN(aV2[0]); PIN(aV2[1]); aO[0] += dz; aO[1] += dz;
        group(3, true);
      }
      group(0, true);
      group(1, false);
    }
    const __amdgpu_buffer_rsrc_t rOut = __builtin_amdgcn_make_buffer_rsrc(out, 0, 2048 * 512 * 4, 0x00020000);
    MFDONE4(yb[0][0], yb[0][1], yb[1][0], yb[1][1]);
    STAMP(12);
#pragma unroll
    for (int u = 0; u < 2; u++) {
      f32x4 o[2];
#pragma unroll
      for (int n = 0; n < 2; n++) o[n] = gt[u] * ya[u][n] + (1.0f - gt[u]) * yb[u][n];
      const bool up = m & 8;
      f32x4 a, b;
#pragma unroll
      for (int r = 0; r < 4; r++) {
        const float t = dpp<0x128>(up ? o[0][r] : o[1][r]);
        a[r] = up ? t : o[0][r];
        b[r] = up ? o[1][r] : t;
      }
      const u32 po = ((64 * i2 + 32 * wr + 4 * u + 8 * ((m >> 2) & 1) + (m & 3)) * 512 + dcol + (up ? 16 : 0)) * 4;
      __builtin_amdgcn_raw_buffer_store_b128(__builtin_bit_cast(u32x4, a), rOut, po, 0, OUTAUX);
      __builtin_amdgcn_raw_buffer_store_b128(__builtin_bit_cast(u32x4, b), rOut, po + 16 * 2048, 0, OUTAUX);
    }
    asm volatile(".Lfused_end_c:");
    STAMP(13);
    __builtin_amdgcn_endpgm();
  } else if (wave >= 2) {
    const int aw = wave - 2, r32 = lane & 31, hf = lane >> 5;
    __builtin_amdgcn_s_setprio(PRIO_A);
    const i32x4 rWS = mkrsrc(ws, WS_END);
    u32x2 ep = ld8_issue(WS_EPOCH + BIAS, rWS);
    if (aw == 1) {
      dma1(sb + L_GW, mkrsrc(gw, 1024), 0, lane * 16 + BIAS);
      touch(sb + L_DUMMY, (const char*)code + min(((idx & 7) * 64 + lane) * 128, code_len));
    } else {
      const int ch = 64 * j2 + lane;
      const float lr = bf2f(lre[ch]), li = bf2f(lim[ch]);
      const float dt = unpk(pk(sp(expf(bf2f(ldt[ch])))))[0];
      float e = expf(dt * lr), s, c;
      sincos_cw(dt * li, s, c);
      const cf lam = {e * c, e * s};
      const float inv = 2.0f / (lr * lr + li * li), nr = lam.re - 1.0f;
      const cf l2 = cmul(lam, lam), l4 = cmul(l2, l2), l8 = cmul(l4, l4), l16 = cmul(l8, l8), l32 = cmul(l16, l16), l64 = cmul(l32, l32);
      cf t = cmul(l64, l64);
      t = cmul(t, t);
      t = cmul(t, t);
      t = cmul(t, t);
      *(LAS f32x4*)(size_t)(sb + L_POW + lane * 16) = f32x4{lam.re, l2.re, lam.im, l2.im};
      *(LAS f32x4*)(size_t)(sb + L_POW + 1024 + lane * 16) = f32x4{l8.re, l8.im, l16.re, l16.im};
      *(LAS f32x4*)(size_t)(sb + L_POW + 2048 + lane * 16) = f32x4{(nr * lr + lam.im * li) * inv, (lam.im * lr - nr * li) * inv, l32.re, l32.im};
      *(LAS f32x4*)(size_t)(sb + L_POW + 3072 + lane * 16) = f32x4{l64.re, l64.im, t.re, t.im};
    }
    WAITV1(0, ep);
    const u64 tag = next_tag(ep[0], ep[1]);
    const u32 tlo = (u32)tag, thi = (u32)(tag >> 32);
#pragma nounroll
    for (int s = 0; s < 5; s++) LBAR();
    {
      const u32 vs = BIAS + WS_STAT + ((i2 * 16 + 8 * hf) * 64 + 32 * aw + r32) * 16;
      u32x4 p[8];
      auto fetch = [&]() {
#pragma unroll
        for (int i = 0; i < 8; i++) p[i] = ld16_issue(vs + i * 1024, rWS);
        WAITV8(p);
        u32 d = 0;
#pragma unroll
        for (int i = 0; i < 8; i++) d |= tagdiff(p[i], tlo, thi);
        return none(d);
      };
      LBAR();
      LBAR();
      if (!fetch()) {
        const u64 t0 = __builtin_amdgcn_s_memrealtime();
        do { STAMP(44 + 10 * aw); } while (!fetch() && __builtin_amdgcn_s_memrealtime() - t0 < SPIN_TICKS);
      }
      auto sum8 = [&](int c) {
        auto two = [&](int i) { return add1(__uint_as_float(p[i][c]), __uint_as_float(p[i + 1][c])); };
        return add1(add1(two(0), two(2)), add1(two(4), two(6)));
      };
      const float s1 = sum8(0), s2 = sum8(2);
      const float mean = halves(s1) * (1.0f / FH);
      const float var = fmaxf(halves(s2) * (1.0f / FH) - mean * mean, 0.f);
      const float rstd = __builtin_amdgcn_rsqf(var + 1e-5f);
      *(LAS float*)(size_t)(sb + L_STAT + hf * 256 + (32 * aw + r32) * 4) = hf ? -mean * rstd : rstd;
      STAMP(45 + 10 * aw);
    }
    LBAR();
    LBAR();
    LBAR();
    const u32 vf1 = BIAS + WS_F1 + j2 * 1024 + lane * 16;
    if (aw == 0) {
      const bool near = g == (kq >> 2);
      spin_flags(near ? vf1 : OOB, rWS, tlo, thi, near);
      STAMP(46);
    }
    __builtin_amdgcn_s_setprio(0);
    LBAR();
    LBAR();
    {
      u32x4 w[2][8];
      int c0 = i2 - 32 + 16 * hf;
      const u32 ve = BIAS + WS_SEG + (j2 * 32 + c0) * 1024 + (32 * aw + r32) * 16;
      auto issue = [&]() {
        PIN(c0);
#pragma unroll
        for (int i = 0; i < 16; i++) w[i >> 3][i & 7] = ld16_issue((ve + i * 1024) | ((c0 + i) & OOB), rWS);
      };
      auto fresh = [&]() {
        WAITV8(w[0]);
        WAITV8(w[1]);
        PIN(c0);
        u32 d = 0;
#pragma unroll
        for (int i = 0; i < 16; i++) d |= tagdiff(w[i >> 3][i & 7], tlo, thi) & ~((c0 + i) >> 31);
        return none(d);
      };
      issue();
      LBAR();
      u32x4 f = ld16_issue(aw == 0 ? vf1 : OOB, rWS);
      LBAR();
      WAITV1(0, f);
      if (aw == 0) {
        if (!flags_ok(f, tlo, thi, true)) { STAMP(51); spin_flags(vf1, rWS, tlo, thi, true); }
        STAMP(49);
        if (blockIdx.x == 0 && lane == 0) __hip_atomic_store((GAS u64*)(ws + WS_EPOCH), tag, __ATOMIC_RELAXED, __HIP_MEMORY_SCOPE_AGENT);
      }
      if (!fresh()) {
        const u64 t0 = __builtin_amdgcn_s_memrealtime();
        do { STAMP(47 + 10 * aw); issue(); } while (!fresh() && __builtin_amdgcn_s_memrealtime() - t0 < SPIN_TICKS);
      }
      STAMP(48 + 10 * aw);
      LBAR();
      const f32x4 pw = ldsf4(sb + L_POW + 3072 + (32 * aw + r32) * 16);
      const cf l64 = {pw[0], pw[1]};
      cf h = {__uint_as_float(w[0][0][0]), __uint_as_float(w[0][0][2])};
#pragma unroll
      for (int i = 1; i < 16; i++) h = cfma(l64, h, cf{__uint_as_float(w[i >> 3][i & 7][0]), __uint_as_float(w[i >> 3][i & 7][2])});
      auto sr = __builtin_amdgcn_permlane32_swap(__float_as_uint(h.re), __float_as_uint(h.re), false, false);
      auto si = __builtin_amdgcn_permlane32_swap(__float_as_uint(h.im), __float_as_uint(h.im), false, false);
      const cf c = cfma(cf{pw[2], pw[3]}, cf{__uint_as_float(sr[0]), __uint_as_float(si[0])}, cf{__uint_as_float(sr[1]), __uint_as_float(si[1])});
      if (hf == 0) *(LAS f32x2*)(size_t)(sb + L_CARRY + (32 * aw + r32) * 8) = f32x2{c.re, c.im};
      STAMP(50 + 10 * aw);
    }
#pragma nounroll
    for (int k = 0; k < 2; k++) {
      const int n = k ? 6 : 9;
      const u32 vf = aw == 1 ? BIAS + (k ? WS_F3 : WS_F2) + i2 * 256 + m * 16 : OOB;
#pragma nounroll
      for (int s = 0; s < n; s++) LBAR();
      LBAR();
      u32x4 f = ld16_issue(vf, rWS);
      LBAR();
      WAITV1(0, f);
      if (aw == 1 && !flags_ok(f, tlo, thi, true)) { STAMP(59); spin_flags(vf, rWS, tlo, thi, true); }
      if (aw == 1) STAMP(61 + k);
    }
#pragma nounroll
    for (int s = 34; s < 42; s++) LBAR();
    __builtin_amdgcn_endpgm();
  } else {
    const int dw = wave;
    LOADER_PRIO;
    const int rowA = 64 * i2 + 32 * dw + g;
    u32 vX[4], vW[6];
#pragma unroll
    for (int j = 0; j < 4; j++) { vX[j] = (rowA + 8 * j) * 1024 + ((m ^ (4 * j + g)) << 4) + BIAS - 1024 * j; PIN(vX[j]); }
#pragma unroll
    for (int n = 0; n < 6; n++) {
      const int c = n * 64 + lane, k = c / 12, cc = c - 12 * k;
      const int o = max((64 * dw + k) * 2752 + 32 * tb - 32 + ((cc ^ (((k >> 3) & 1) << 1)) << 4), 0);
      vW[n] = narrow && cc >= 8 ? OOB : o + BIAS - 1024 * (n & 3);
      PIN(vW[n]);
    }
    __builtin_amdgcn_sched_barrier(0);
    const i32x4 rX = mkrsrc(x, 2048 * 512 * 2), rW = mkrsrc(win, 512 * 1376 * 2), rWS = mkrsrc(ws, WS_END);
    u32 na = sb, nz = sb + RZ;
    auto rot = [&]() { na += SA; nz += SZ; if (na == sb + 3 * SA) na = sb; if (nz == sb + RZ + 3 * SZ) nz = sb + RZ; };
    auto rows = [&](u32 lds, i32x4 rs, u32 soff, u32 second, const u32* v) { dma4(lds, rs, soff, v); dma4(lds + 4096, rs, soff + second, v); };
    auto issueX = [&](int s) { rows(na + dw * 8192, rX, (s ^ kx) * 256, 4 * 1024, vX); };
    auto issueW = [&](u32 base, int s, u32 col) {
#pragma unroll
      for (int q = 0; q < 2; q++) {
        const u32 l = base + dw * 12288 + q * 6144, o = (s ^ kx) * (128 * 2752) + q * (32 * 2752) + col;
        dma4(l, rW, o, vW);
        dma2(l + 4096, rW, o, vW + 4);
      }
    };
#pragma nounroll
    for (int s = 0; s < 2; s++) { issueX(s); issueW(nz, s, 2 * FH); rot(); }
    LSTAMP(42);
    const i32x4 rB = mkrsrc(dw ? bim : bre, 512 * 512 * 2), rA = mkrsrc(wsp, 2048 * 2048 * 2);
    u32 vB[4], vA[4];
#pragma unroll
    for (int j = 0; j < 4; j++) {
      vB[j] = (64 * j2 + 4 * j + g) * 1024 + ((m ^ (4 * j + g)) << 4) + BIAS - 1024 * j;
      vA[j] = (rowA + 8 * j) * 4096 + ((m ^ (4 * j + g)) << 4) + BIAS - 1024 * j;
      PIN(vB[j]); PIN(vA[j]);
    }
    u32 vZ = BIAS + WS_Z + (2 * dw * 48 + tb) * 1024 + lane * 16, vZ2 = narrow ? OOB : vZ;
    PIN(vZ); PIN(vZ2);
    WAITV(20);
#pragma nounroll
    for (int s = 0; s < 2; s++) { LBAR(); LSTAMP(s); issueX(s + 2); issueW(nz, s + 2, 2 * FH); rot(); WAITV(20); }
    auto issueB = [&](int s) {
      const u32 l = sb + uslot(s) + dw * 16384;
      rows(l, rB, (s ^ kx) * 256, 16 * 1024, vB);
      rows(l + 8192, rB, (s ^ kx) * 256 + 32 * 1024, 16 * 1024, vB);
    };
    auto kst = [&](int s) { return ((s + kq) & 15) ^ (s < 4 ? kx & KXG0 : kx); };
    auto issueA = [&](u32 base, int s) { rows(base + dw * 8192, rA, kst(s) * 256, 4 * 4096, vA); };
    auto issueZ = [&](u32 base, int s) {
#pragma unroll
      for (int q = 0; q < 2; q++) {
        const u32 l = base + dw * 12288 + q * 6144, o = kst(s) * (4 * 48 * 1024) + q * (48 * 1024);
        dma4c(l, rWS, o, vZ);
        dma2c(l + 4096, rWS, o + 4096, vZ2);
      }
    };
    LBAR(); LSTAMP(2); issueB(0); WAITV(16);
    LBAR(); LSTAMP(3); issueB(1); issueA(sb + SA, 0); issueA(sb + 2 * SA, 1); WAITV(32);
    LBAR(); LSTAMP(4); issueB(2); WAITV(32);
    LBAR(); LSTAMP(5); issueB(3);
    const i32x4 rC0 = mkrsrc(cre, 512 * 512 * 2), rC1 = mkrsrc(cim, 512 * 512 * 2), rO = mkrsrc(wout, FH * 512 * 2);
    u32 vC[4], vO[4], vE[4];
#pragma unroll
    for (int j = 0; j < 4; j++) {
      vC[j] = (64 * j2 + 32 * dw + 4 * j + g) * 1024 + ((m ^ (4 * j + g)) << 4) + BIAS - 1024 * j;
      const int row = 64 * dw + 8 * j + (lane >> 3);
      vO[j] = row * 1024 + j2 * 128 + (((lane & 7) ^ (((row >> 1) & 3) << 1)) << 4) + BIAS - 1024 * j;
      const int mm = 8 * (j & 1) + (lane >> 3);
      vE[j] = (64 * i2 + 32 * dw + 4 * (j >> 1) + 8 * (mm >> 2) + (mm & 3)) * 1024 + j2 * 128 + (((lane & 7) ^ (mm >> 1)) << 4) + BIAS - 1024 * j;
    }
    u32 vH = BIAS + WS_H + (2 * i2 + dw) * 32 * 1024 + lane * 16;
    u32 vV = BIAS + WS_V + ((2 * i2 + dw) * 48 + 1) * 1024 + lane * 16;
    WAITV(16);
    LBAR(); LSTAMP(6);
#pragma unroll
    for (int j = 0; j < 4; j++) { PIN(vC[j]); PIN(vO[j]); PIN(vE[j]); }
    PIN(vH); PIN(vV);
    __builtin_amdgcn_s_sleep(SLP6); issueW(sb + wslot(0), 0, 0); WAITV(12);
    LBAR(); LSTAMP(7); __builtin_amdgcn_s_sleep(SLP7); issueW(sb + wslot(1), 1, 0); WAITV(12);
    LBAR(); LSTAMP(8); issueW(sb + wslot(2), 2, 0); WAITV(12);
    LBAR(); LSTAMP(9); issueW(sb + wslot(3), 3, 0); WAITV(12);
    LBAR(); LSTAMP(10); issueZ(sb + RZ, 0); issueZ(sb + RZ + SZ, 1); WAITV(24);
    LBAR(); LSTAMP(11); issueA(sb, 2); dma4(sb + LX + dw * 4096, rX, 0, vE); WAITV(24);
    LBAR(); LSTAMP(12); issueZ(sb + RZ + 2 * SZ, 2); WAITV(24);
    na = sb + SA; nz = sb + RZ;
#pragma nounroll
    for (int s = 3; s < 16; s++) { LBAR(); LSTAMP(10 + s); issueZ(nz, s); issueA(na, s); rot(); WAITV(20); }
    auto frags = [&](u32 soff, u32 v) { dma4c(na + dw * 8192, rWS, soff, v); dma4c(na + dw * 8192 + 4096, rWS, soff + 4096, v); };
#pragma nounroll
    for (int s = 0; s < 4; s++) {
      LBAR(); LSTAMP(26 + s);
      frags((s ^ kx) * 8192, vH);
      rows(nz + dw * 8192, rC0, (s ^ kx) * 256, 16 * 1024, vC);
      rot(); WAITV(16);
    }
#pragma nounroll
    for (int s = 0; s < 4; s++) {
      LBAR(); LSTAMP(30 + s);
      frags(64 * 32 * 1024 + (s ^ kx) * 8192, vH);
      rows(nz + dw * 8192, rC1, (s ^ kx) * 256, 16 * 1024, vC);
      rot(); WAITV(16);
    }
#pragma nounroll
    for (int s = 0; s < 5; s++) {
      const int t = s < 4 ? s ^ (kx & KXV) : s;
      LBAR(); LSTAMP(34 + s);
      frags(t * 8192, vV);
      rows(nz + dw * 8192, rO, t * (128 * 1024), 32 * 1024, vO);
      rot(); WAITV(16);
    }
    LBAR(); LSTAMP(39);
    dma4c(na + dw * 8192, rWS, 5 * 8192, vV);
    dma4(nz + dw * 4096, rO, 5 * (128 * 1024) - dw * (32 * 1024), vO);
    WAITV(8);
    LBAR(); LSTAMP(40);
    WAITV(0);
    LBAR(); LSTAMP(41);
    asm volatile(".Lfused_end_l:");
    __builtin_amdgcn_endpgm();
  }
}
}

extern "C" void kernel_launch(void* const* d_in, const int* in_sizes, int n_in, void* d_out, int out_size, void* d_ws, size_t ws_size,
                              hipStream_t stream) {
  (void)hipFuncSetAttribute((const void*)fused_kernel, hipFuncAttributeMaxDynamicSharedMemorySize, LDS_BYTES);
  fused_kernel<<<256, 512, LDS_BYTES, stream>>>((const u16*)d_in[0], (const u16*)d_in[1], (const u16*)d_in[2], (const u16*)d_in[3],
                                                (const u16*)d_in[4], (const u16*)d_in[5], (const u16*)d_in[6], (const u16*)d_in[7],
                                                (const u16*)d_in[8], (const u16*)d_in[10], (const u16*)d_in[11], (const u16*)d_in[12],
                                                (const u16*)d_in[13], (const u16*)d_in[14], (float*)d_out, (char*)d_ws);
}
```
